```python
import torch

def _softmax(x, axis=-1):
    return torch.softmax(x, dim=axis)
def _gelu(x):
    return torch.nn.functional.gelu(x, approximate="tanh")
def _silu(x):
    return torch.nn.functional.silu(x)
def _sigmoid(x):
    return torch.sigmoid(x)
def _layernorm(x, axis=-1, eps=1e-5):
    m = x.mean(dim=axis, keepdim=True)
    v = x.var(dim=axis, keepdim=True, unbiased=False)
    return (x - m) / torch.sqrt(v + eps)
def _scatter_add(tgt, idx, src, axis=0):
    return tgt.clone().index_add_(axis, idx, src)
def _segment_sum(src, seg, n):
    out = torch.zeros((n,) + src.shape[1:], dtype=src.dtype)
    return out.index_add_(0, seg, src)
def _pad(x, pw):
    flat = tuple(v for pair in reversed(pw) for v in pair)
    return torch.nn.functional.pad(x, flat)

kernel_name = 'sequential_preLN__compressed_kv_cla__dense_activation_gelu__decode'

def setup_inputs():

    _w = (lambda a: (lambda t: t.to(torch.bfloat16) if t.is_floating_point() else t)(torch.as_tensor(a)))
    def _A_setup():
        B, H, Sq, Sk, D, M = 1, 32, 1, 16384, 128, 512
        Dc = 4 * D
        return {
            "x": _w((lambda *s: torch.randn(*s, dtype=torch.bfloat16) * 0.05)(B, Sq, M)),
            "c_kv": _w((lambda *s: torch.randn(*s, dtype=torch.bfloat16) * 0.8)(B, Sk, Dc)),
            "w_q": _w((lambda *s: torch.randn(*s, dtype=torch.bfloat16) * 3.2)(M, H, D) * (M ** -0.5)),
            "w_uk": _w((lambda *s: torch.randn(*s, dtype=torch.bfloat16) * 3.2)(Dc, H, D) * (Dc ** -0.5)),
            "w_uv": _w((lambda *s: torch.randn(*s, dtype=torch.bfloat16) * 0.8)(Dc, H, D) * (Dc ** -0.5)),
            "w_o": _w((lambda *s: torch.randn(*s, dtype=torch.bfloat16) * 0.8)(H, D, M) * ((H * D) ** -0.5)),
        }
    _A_in = _A_setup()

    def _B_setup():
        B, S, D, F = 1, 1, 512, 1792
        return {
            "x": _w((lambda *s: torch.randn(*s, dtype=torch.bfloat16) * 0.05)(B, S, D)),
            "w1": _w((lambda *s: torch.randn(*s, dtype=torch.bfloat16) * 3.2)(D, F) * (D ** -0.5)),
            "b1": _w((lambda *s: torch.randn(*s, dtype=torch.bfloat16) * 3.2)(F) * 0.01),
            "w2": _w((lambda *s: torch.randn(*s, dtype=torch.bfloat16) * 0.8)(F, D) * (F ** -0.5)),
            "b2": _w((lambda *s: torch.randn(*s, dtype=torch.bfloat16) * 0.8)(D) * 0.01),
        }
    _B_in = _B_setup()

    return {
        "A_x": _A_in["x"],
        "A_c_kv": _A_in["c_kv"],
        "A_w_q": _A_in["w_q"],
        "A_w_uk": _A_in["w_uk"],
        "A_w_uv": _A_in["w_uv"],
        "A_w_o": _A_in["w_o"],
        "B_x": _B_in["x"],
        "B_w1": _B_in["w1"],
        "B_b1": _B_in["b1"],
        "B_w2": _B_in["w2"],
        "B_b2": _B_in["b2"],
    }

def reference(A_x, A_c_kv, A_w_q, A_w_uk, A_w_uv, A_w_o, B_x, B_w1, B_b1, B_w2, B_b2):
    def _A_ref(x, c_kv, w_q, w_uk, w_uv, w_o):
        B, Sq, M = x.shape[0], x.shape[1], x.shape[2]
        Sk = c_kv.shape[1]
        H, D = w_q.shape[1], w_q.shape[2]
        q = torch.einsum("bqm,mhd->bhqd", x, w_q)
        k = torch.einsum("bkc,chd->bhkd", c_kv, w_uk)
        v = torch.einsum("bkc,chd->bhkd", c_kv, w_uv)
        scores = torch.einsum("bhqd,bhkd->bhqk", q, k) * (D ** -0.5)
        off = Sk - Sq
        qi = torch.arange(Sq)[:, None]
        kj = torch.arange(Sk)[None, :]
        cmask = kj <= (qi + off)
        scores = torch.where(cmask[None, None], scores, -1e9)
        p = _softmax(scores, axis=-1)
        ctx = torch.einsum("bhqk,bhkd->bhqd", p, v)
        return torch.einsum("bhqd,hdm->bqm", ctx, w_o)

    def _B_ref(x, w1, b1, w2, b2):
        h = torch.einsum("bsd,df->bsf", x, w1) + b1
        h = _gelu(h)
        return torch.einsum("bsf,fd->bsd", h, w2) + b2

    x = A_x
    h = x + _A_ref(_layernorm(x, axis=-1), A_c_kv, A_w_q, A_w_uk, A_w_uv, A_w_o)
    h2 = h + _B_ref(_layernorm(h, axis=-1), B_w1, B_b1, B_w2, B_b2)
    return h2
```

```cpp
#include <hip/hip_runtime.h>
#include <hip/hip_bf16.h>
typedef unsigned short u16;
__device__ __forceinline__ float bf2f(u16 b){ return __uint_as_float((unsigned)b<<16); }
__device__ __forceinline__ u16 f2bf(float f){ unsigned u=__float_as_uint(f); u += 0x7fffu + ((u>>16)&1u); return (u16)(u>>16); }

constexpr int M=512,H=32,D=128,DC=512,SK=16384,F=1792;

__device__ float block_sum(float v, float* red){
  for(int o=32;o>0;o>>=1) v += __shfl_xor(v,o);
  int w=threadIdx.x>>6, nw=blockDim.x>>6;
  __syncthreads();
  if((threadIdx.x&63)==0) red[w]=v;
  __syncthreads();
  float s=0; for(int i=0;i<nw;i++) s+=red[i];
  return s;
}
__global__ void k_q(const u16* x,const u16* wq,float* q){
  __shared__ float xn[M]; __shared__ float red[16]; __shared__ float part[4][128];
  int t=threadIdx.x; float v=bf2f(x[t]);
  float mean=block_sum(v,red)/M; float d0=v-mean; float var=block_sum(d0*d0,red)/M;
  xn[t]=d0/sqrtf(var+1e-5f); __syncthreads();
  int h=blockIdx.x, d=t&127, s=t>>7; float a=0;
  for(int m=s*128;m<s*128+128;m++) a+=xn[m]*bf2f(wq[(size_t)m*H*D+h*D+d]);
  part[s][d]=a; __syncthreads();
  if(t<128) q[h*D+t]=part[0][t]+part[1][t]+part[2][t]+part[3][t];
}
__global__ void k_qabs(const float* q,const u16* wuk,float* qa){
  int h=blockIdx.x, c=threadIdx.x; float a=0;
  for(int d=0;d<D;d++) a+=q[h*D+d]*bf2f(wuk[(size_t)c*H*D+h*D+d]);
  qa[h*DC+c]=a*0.08838834764831845f;
}
__global__ void k_scores(const float* qa,const u16* ckv,float* sc){
  int k=blockIdx.x*8+(threadIdx.x>>5), h=threadIdx.x&31; float a=0;
  for(int c=0;c<DC;c++) a+=qa[h*DC+c]*bf2f(ckv[(size_t)k*DC+c]);
  sc[(size_t)h*SK+k]=a;
}
__global__ void k_softmax(float* sc){
  __shared__ float red[16];
  int h=blockIdx.x; float m=-1e30f;
  for(int k=threadIdx.x;k<SK;k+=blockDim.x) m=fmaxf(m,sc[(size_t)h*SK+k]);
  for(int o=32;o>0;o>>=1) m=fmaxf(m,__shfl_xor(m,o));
  if((threadIdx.x&63)==0) red[threadIdx.x>>6]=m; __syncthreads();
  m=red[0]; for(int i=1;i<(int)(blockDim.x>>6);i++) m=fmaxf(m,red[i]);
  float s=0;
  for(int k=threadIdx.x;k<SK;k+=blockDim.x){ float e=__expf(sc[(size_t)h*SK+k]-m); sc[(size_t)h*SK+k]=e; s+=e; }
  s=block_sum(s,red);
  for(int k=threadIdx.x;k<SK;k+=blockDim.x) sc[(size_t)h*SK+k]/=s;
}
__global__ void k_pv(const float* p,const u16* ckv,float* op){
  int h=blockIdx.x, ch=blockIdx.y, c=threadIdx.x; float a=0;
  for(int k=ch*512;k<ch*512+512;k++) a+=p[(size_t)h*SK+k]*bf2f(ckv[(size_t)k*DC+c]);
  op[((size_t)ch*H+h)*DC+c]=a;
}
__global__ void k_ctx(const float* op,const u16* wuv,float* ctx){
  __shared__ float o[DC]; __shared__ float part[4][128];
  int h=blockIdx.x,t=threadIdx.x; float a=0; for(int ch=0;ch<32;ch++) a+=op[((size_t)ch*H+h)*DC+t];
  o[t]=a; __syncthreads();
  int d=t&127,s=t>>7; a=0; for(int c=s*128;c<s*128+128;c++) a+=o[c]*bf2f(wuv[(size_t)c*H*D+h*D+d]);
  part[s][d]=a; __syncthreads();
  if(t<128) ctx[h*D+t]=part[0][t]+part[1][t]+part[2][t]+part[3][t];
}
__global__ void k_o(const float* ctx,const u16* wo,float* po){
  int h=blockIdx.x,m=threadIdx.x; float a=0; for(int d=0;d<D;d++) a+=ctx[h*D+d]*bf2f(wo[((size_t)h*D+d)*M+m]);
  po[h*M+m]=a;
}
__global__ void k_ffn1(const u16* x,const float* po,const u16* w1,const u16* b1,float* hbuf,float* g){
  __shared__ float hn[M]; __shared__ float red[16]; __shared__ float part[4][128];
  int t=threadIdx.x; float v=0; for(int h=0;h<32;h++) v+=po[h*M+t]; v+=bf2f(x[t]);
  if(blockIdx.x==0) hbuf[t]=v;
  float mean=block_sum(v,red)/M; float d0=v-mean; float var=block_sum(d0*d0,red)/M;
  hn[t]=d0/sqrtf(var+1e-5f); __syncthreads();
  int f=blockIdx.x*128+(t&127), s=t>>7; float a=0;
  for(int m=s*128;m<s*128+128;m++) a+=hn[m]*bf2f(w1[(size_t)m*F+f]);
  part[s][t&127]=a; __syncthreads();
  if(t<128){ float u=part[0][t]+part[1][t]+part[2][t]+part[3][t]+bf2f(b1[f]);
    float z=0.7978845608028654f*(u+0.044715f*u*u*u); g[f]=0.5f*u*(1.f+tanhf(z)); }
}
__global__ void k_ffn2(const float* hbuf,const float* g,const u16* w2,const u16* b2,u16* out){
  __shared__ float part[8][64];
  int t=threadIdx.x, m=blockIdx.x*64+(t&63), s=t>>6; float a=0;
  for(int f=s*224;f<s*224+224;f++) a+=g[f]*bf2f(w2[(size_t)f*M+m]);
  part[s][t&63]=a; __syncthreads();
  if(t<64){ float u=0; for(int i=0;i<8;i++) u+=part[i][t]; out[m]=f2bf(hbuf[m]+u+bf2f(b2[m])); }
}
extern "C" void kernel_launch(void* const* d_in, const int* in_sizes, int n_in, void* d_out, int out_size, void* d_ws, size_t ws_size, hipStream_t stream){
  const u16* x=(const u16*)d_in[0]; const u16* ckv=(const u16*)d_in[1]; const u16* wq=(const u16*)d_in[2]; const u16* wuk=(const u16*)d_in[3];
  const u16* wuv=(const u16*)d_in[4]; const u16* wo=(const u16*)d_in[5]; const u16* w1=(const u16*)d_in[7]; const u16* b1=(const u16*)d_in[8];
  const u16* w2=(const u16*)d_in[9]; const u16* b2=(const u16*)d_in[10];
  float* ws=(float*)d_ws; float* q=ws; float* qa=q+4096; float* sc=qa+H*DC; float* op=sc+(size_t)H*SK; float* ctx=op+32*H*DC; float* po=ctx+4096; float* hb=po+H*M; float* g=hb+M;
  k_q<<<32,512,0,stream>>>(x,wq,q);
  k_qabs<<<32,512,0,stream>>>(q,wuk,qa);
  k_scores<<<SK/8,256,0,stream>>>(qa,ckv,sc);
  k_softmax<<<32,1024,0,stream>>>(sc);
  k_pv<<<dim3(32,32),512,0,stream>>>(sc,ckv,op);
  k_ctx<<<32,512,0,stream>>>(op,wuv,ctx);
  k_o<<<32,512,0,stream>>>(ctx,wo,po);
  k_ffn1<<<14,512,0,stream>>>(x,po,w1,b1,hb,g);
  k_ffn2<<<8,512,0,stream>>>(hb,g,w2,b2,(u16*)d_out);
}
```

```cpp
#include <hip/hip_runtime.h>
#include <hip/hip_bf16.h>

#define GAS __attribute__((address_space(1)))
#define LAS __attribute__((address_space(3)))
#define DI __device__ __forceinline__
typedef unsigned short u16;
typedef unsigned long long u64;
typedef unsigned v4u __attribute__((ext_vector_type(4)));
typedef unsigned v2u __attribute__((ext_vector_type(2)));
typedef float f32x4 __attribute__((ext_vector_type(4)));
typedef float f32x2 __attribute__((ext_vector_type(2)));
typedef short bf16x8 __attribute__((ext_vector_type(8)));
typedef short s16x4 __attribute__((ext_vector_type(4)));
typedef __bf16 bf16x2 __attribute__((ext_vector_type(2)));

constexpr int WS_EPOCH = 0;
constexpr int WS_Q1 = 4096;
constexpr int WS_Q2 = WS_Q1 + 262144;
constexpr int WS_ML = WS_Q2 + 65536;
constexpr int WS_A2 = WS_ML + 32768;
constexpr int WS_B = WS_A2 + 65536;
constexpr int WS_C = WS_B + 16384;
constexpr int WS_A = 1 << 20;
constexpr int WS_BYTES = WS_A + (4 << 20);
constexpr int CP = 1040;
constexpr int L_C = 0;
constexpr int L_Q = 128 * CP;
constexpr int L_P = L_Q + 16 * CP;
constexpr int PP = 272;
constexpr int L_MX = L_P + 16 * PP;
constexpr int L_LS = L_MX + 256;
constexpr int L_BN = L_LS + 1024;
constexpr int L_WG = L_BN + 2048;
constexpr int L_PART = L_WG + 512;
constexpr int L_STAT = L_PART + 4096;
constexpr int L_BYTES = L_STAT + 64;
static_assert(L_BYTES <= 163840, "LDS");
constexpr unsigned SENT = 0xFFFFFFFFu;
constexpr unsigned SPIN_CAP = 1u << 15;
constexpr int INFL = 8;

DI float bflo(unsigned u) { return __builtin_bit_cast(float, u << 16); }
DI float bfhi(unsigned u) { return __builtin_bit_cast(float, u & 0xFFFF0000u); }
DI unsigned pk2(float a, float b) { f32x2 f = {a, b}; return __builtin_bit_cast(unsigned, __builtin_convertvector(f, bf16x2)); }
DI float rbf(float a) { return bflo(pk2(a, a)); }
DI unsigned fu(float a) { return __builtin_bit_cast(unsigned, a); }
DI float uf(unsigned a) { return __builtin_bit_cast(float, a); }
DI unsigned nosent(unsigned a) { return a < 0xFFFFFFFEu ? a : 0xFFFFFFFEu; }
DI f32x4 mfma(v4u a, v4u b, f32x4 c) { return __builtin_amdgcn_mfma_f32_16x16x32_bf16(__builtin_bit_cast(bf16x8, a), __builtin_bit_cast(bf16x8, b), c, 0, 0, 0); }
DI float dot2(unsigned a, unsigned b, float c) { return __builtin_amdgcn_fdot2_f32_bf16(__builtin_bit_cast(bf16x2, a), __builtin_bit_cast(bf16x2, b), c, false); }
DI float rs32(float a, float b) { auto r = __builtin_amdgcn_permlane32_swap(fu(a), fu(b), false, false); return uf(r[0]) + uf(r[1]); }
DI float rs16(float a, float b) { auto r = __builtin_amdgcn_permlane16_swap(fu(a), fu(b), false, false); return uf(r[0]) + uf(r[1]); }
DI float mx32(float a) { auto r = __builtin_amdgcn_permlane32_swap(fu(a), fu(a), false, false); return fmaxf(uf(r[0]), uf(r[1])); }
DI float mx16(float a) { auto r = __builtin_amdgcn_permlane16_swap(fu(a), fu(a), false, false); return fmaxf(uf(r[0]), uf(r[1])); }
template <int CTRL> DI float dpp(float a) { return uf((unsigned)__builtin_amdgcn_update_dpp(0, (int)fu(a), CTRL, 0xF, 0xF, false)); }
DI float row_sum(float a) { a += dpp<0x128>(a); a += dpp<0x124>(a); a += dpp<0x122>(a); a += dpp<0x121>(a); return a; }
DI float row_max(float a) { a = fmaxf(a, dpp<0x128>(a)); a = fmaxf(a, dpp<0x124>(a)); a = fmaxf(a, dpp<0x122>(a)); a = fmaxf(a, dpp<0x121>(a)); return a; }
DI float wave_sum(float a) { a = row_sum(a); a = rs16(a, a); return rs32(a, a); }
DI float wave_max(float a) { return mx32(mx16(row_max(a))); }
DI v4u ldnt(const void* p) { return __builtin_nontemporal_load((const GAS v4u*)p); }
DI v4u ld16(__amdgpu_buffer_rsrc_t rs, int off) { return __builtin_amdgcn_raw_buffer_load_b128(rs, off, 0, 16); }
DI v2u ld8(__amdgpu_buffer_rsrc_t rs, int off) { return __builtin_amdgcn_raw_buffer_load_b64(rs, off, 0, 16); }
DI unsigned ld4(__amdgpu_buffer_rsrc_t rs, int off) { return __builtin_amdgcn_raw_buffer_load_b32(rs, off, 0, 16); }
DI void st16(__amdgpu_buffer_rsrc_t rs, int off, v4u v) { __builtin_amdgcn_raw_buffer_store_b128(v, rs, off, 0, 16); }
DI void st8(__amdgpu_buffer_rsrc_t rs, int off, v2u v) { __builtin_amdgcn_raw_buffer_store_b64(v, rs, off, 0, 16); }
DI void st4(__amdgpu_buffer_rsrc_t rs, int off, unsigned v) { __builtin_amdgcn_raw_buffer_store_b32(v, rs, off, 0, 16); }
DI unsigned umax4(v4u v) { return max(max(v.x, v.y), max(v.z, v.w)); }
DI void bar() { asm volatile("s_waitcnt lgkmcnt(0)\n\ts_barrier" ::: "memory"); }
#define FENCE() asm volatile("" ::: "memory")
#define VMWAIT(N) asm volatile("s_waitcnt vmcnt(%0)" ::"i"(N) : "memory")
template <bool NT> DI void dma16(const void* base, unsigned voff, unsigned lds) {
  unsigned keep;
  if (NT) asm volatile("s_mov_b32 %0, m0\n\ts_mov_b32 m0, %3\n\ts_nop 0\n\tglobal_load_lds_dwordx4 %1, %2 nt\n\ts_mov_b32 m0, %0" : "=&s"(keep) : "v"(voff), "s"(base), "s"(lds) : "memory");
  else asm volatile("s_mov_b32 %0, m0\n\ts_mov_b32 m0, %3\n\ts_nop 0\n\tglobal_load_lds_dwordx4 %1, %2\n\ts_mov_b32 m0, %0" : "=&s"(keep) : "v"(voff), "s"(base), "s"(lds) : "memory");
}
DI v4u lds16(LAS unsigned char* L, int off) { return *(LAS v4u*)(L + off); }
DI v4u trfrag(LAS unsigned char* L, int off0, int off1) {
  s16x4 a = __builtin_amdgcn_ds_read_tr16_b64_v4i16((LAS s16x4*)(L + off0));
  s16x4 b = __builtin_amdgcn_ds_read_tr16_b64_v4i16((LAS s16x4*)(L + off1));
  v2u x = __builtin_bit_cast(v2u, a), y = __builtin_bit_cast(v2u, b);
  return v4u{x.x, x.y, y.x, y.y};
}
#define STAMP(k)

__global__ void __launch_bounds__(512, 2)
fused_block(const u16* __restrict__ xin, const u16* __restrict__ ckv, const u16* __restrict__ wq, const u16* __restrict__ wuk,
            const u16* __restrict__ wuv, const u16* __restrict__ wo, const u16* __restrict__ w1, const u16* __restrict__ b1,
            const u16* __restrict__ w2, const u16* __restrict__ b2, u16* __restrict__ out, unsigned char* ws) {
  extern __shared__ __attribute__((aligned(16))) unsigned char smem[];
  LAS unsigned char* L = (LAS unsigned char*)smem;
  const unsigned lbase = (unsigned)(size_t)L;
  const int b = blockIdx.x, lane = threadIdx.x & 63, w = __builtin_amdgcn_readfirstlane(threadIdx.x >> 6);
  const int x = b & 7, y = b >> 3, hg = y & 1, i = y >> 1;
  const int n = i * 8 + x;
  const int g = lane >> 4, r = lane & 15;
  const __amdgpu_buffer_rsrc_t rs = __builtin_amdgcn_make_buffer_rsrc(ws, 0, WS_BYTES, 0x00020000);
  const bool is_head = i < 8, is_wo = (i >> 2) == 2;
  const int e = ((i - 12) << 1) | hg;
  const bool is_f2 = i >= 12 && e < 4;
  const int f1 = (e - 4) * 8 + x;
  const bool is_f1 = i >= 12 && e >= 4 && f1 < 28;
  const int hh = 16 * hg + 2 * x + (i >> 2), q4 = i & 3;
  const int cg = ((i - 8) << 1) | hg;
  STAMP(0);

  unsigned tag = 0;
  v4u wv[8];
  if (w < 4) {
    const int h = 16 * hg + 2 * x + (i >> 3), j = i & 7;
    const int rsub = lane >> 2, ch = lane & 3;
    v4u wqv[4];
    const char* wqp = (const char*)wq + (size_t)(64 * j + 4 * rsub) * 8192 + h * 256 + w * 64 + ch * 16;
#pragma unroll
    for (int ii = 0; ii < 4; ii++) wqv[ii] = ldnt(wqp + ii * 8192);
    const v4u xa = *(const GAS v4u*)((const char*)xin + lane * 16);
    const v2u xr = *(const GAS v2u*)((const char*)xin + (64 * j + 4 * rsub) * 2);
    tag = ld4(rs, WS_EPOCH) + 1u;
    v4u kf[2][4];
    if (w < 2) {
      const char* kp = (const char*)wuk + (size_t)(64 * j + 32 * w + r) * 8192 + h * 256 + g * 16;
#pragma unroll
      for (int T = 0; T < 2; T++)
#pragma unroll
        for (int s = 0; s < 4; s++) kf[T][s] = ldnt(kp + T * 16 * 8192 + s * 64);
      const v4u sv = {SENT, SENT, SENT, SENT};
#pragma unroll
      for (int k = 0; k < 8; k++) st16(rs, WS_A + (hg * 128 + n) * 16384 + (w * 8 + k) * 1024 + lane * 16, sv);
      if (w == 0) {
        if (lane < 8) st16(rs, WS_ML + (hg * 128 + n) * 128 + lane * 16, sv);
        if (is_head) { if (lane < 32) st16(rs, WS_A2 + (hh * 4 + q4) * 512 + lane * 16, sv); }
        else if (is_wo) { if (lane < 16) st16(rs, WS_B + (cg * 8 + x) * 256 + lane * 16, sv); }
        else if (is_f1) { if (lane < 8) st16(rs, WS_C + f1 * 128 + lane * 16, sv); }
      }
    }
    __builtin_amdgcn_sched_barrier(0);
    float xv[8] = {bflo(xa.x), bfhi(xa.x), bflo(xa.y), bfhi(xa.y), bflo(xa.z), bfhi(xa.z), bflo(xa.w), bfhi(xa.w)};
    const float x0 = uf(__builtin_amdgcn_readfirstlane(fu(xv[0])));
    float s1 = 0.f, s2 = 0.f;
#pragma unroll
    for (int k = 0; k < 8; k++) { const float d = xv[k] - x0; s1 += d; s2 += d * d; }
    s1 = wave_sum(s1); s2 = wave_sum(s2);
    const float md = s1 * (1.f / 512.f);
    const float mean = rbf(x0 + md);
    const float var = rbf(fmaxf(s2 * (1.f / 512.f) - md * md, 0.f));
    const float den = rbf(sqrtf(rbf(var + 1e-5f)));
    const unsigned xn01 = pk2(rbf(bflo(xr.x) - mean) / den, rbf(bfhi(xr.x) - mean) / den);
    const unsigned xn23 = pk2(rbf(bflo(xr.y) - mean) / den, rbf(bfhi(xr.y) - mean) / den);
    float a[8];
#pragma unroll
    for (int k = 0; k < 4; k++) {
      a[2 * k] = dot2(__builtin_amdgcn_perm(wqv[1][k], wqv[0][k], 0x05040100), xn01, dot2(__builtin_amdgcn_perm(wqv[3][k], wqv[2][k], 0x05040100), xn23, 0.f));
      a[2 * k + 1] = dot2(__builtin_amdgcn_perm(wqv[1][k], wqv[0][k], 0x07060302), xn01, dot2(__builtin_amdgcn_perm(wqv[3][k], wqv[2][k], 0x07060302), xn23, 0.f));
    }
    STAMP(1);
    float c0 = rs32(a[0], a[4]), c1 = rs32(a[1], a[5]), c2 = rs32(a[2], a[6]), c3 = rs32(a[3], a[7]);
    float d0 = rs16(c0, c2), d1 = rs16(c1, c3);
    d0 += dpp<0x128>(d0); d1 += dpp<0x128>(d1);
    d0 += dpp<0x124>(d0); d1 += dpp<0x124>(d1);
    tag = __builtin_amdgcn_readfirstlane(tag);
    if ((lane & 12) == 0) st16(rs, WS_Q1 + (h * 8 + j) * 1024 + (16 * w + 4 * ch + g) * 16, v4u{fu(d0), tag, fu(d1), tag});
    STAMP(2);
    if (w < 2) {
      float q0, q1;
      for (unsigned sp = 0;; sp++) {
        FENCE();
        v4u p[8];
#pragma unroll
        for (int k = 0; k < 8; k++) p[k] = ld16(rs, WS_Q1 + (h * 8 + k) * 1024 + lane * 16);
        bool ok = true; q0 = 0.f; q1 = 0.f;
#pragma unroll
        for (int k = 0; k < 8; k++) { ok &= (p[k].y == tag) & (p[k].w == tag); q0 += uf(p[k].x); q1 += uf(p[k].z); }
        if (__all(ok) || sp > SPIN_CAP) break;
      }
      STAMP(3);
      *(LAS unsigned*)(L + L_BN + w * 256 + lane * 4) = pk2(q0, q1);
      FENCE();
      f32x4 acc[2] = {{0.f, 0.f, 0.f, 0.f}, {0.f, 0.f, 0.f, 0.f}};
#pragma unroll
      for (int s = 0; s < 4; s++) {
        const v4u qf = lds16(L, L_BN + w * 256 + s * 64 + g * 16);
        acc[0] = mfma(kf[0][s], qf, acc[0]);
        acc[1] = mfma(kf[1][s], qf, acc[1]);
      }
      const f32x4 o = (r & 1) ? acc[1] : acc[0];
      if (r < 2) st16(rs, WS_Q2 + (h * 8 + j) * 256 + ((2 * w + r) * 4 + g) * 16, v4u{pk2(o.x, o.y), tag, pk2(o.z, o.w), tag});
      STAMP(4);
    }
    for (unsigned sp = 0;; sp++) {
      FENCE();
      v4u p[8];
#pragma unroll
      for (int k = 0; k < 8; k++) p[k] = ld16(rs, WS_Q2 + hg * 32768 + (w * 8 + k) * 1024 + lane * 16);
      bool ok = true;
#pragma unroll
      for (int k = 0; k < 8; k++) {
        ok &= (p[k].y == tag) & (p[k].w == tag);
        *(LAS v2u*)(L + L_Q + (4 * w + (k >> 1)) * CP + (k & 1) * 512 + lane * 8) = v2u{p[k].x, p[k].z};
      }
      if (__all(ok) || sp > SPIN_CAP) break;
    }
    STAMP(5);
    bar();
    v4u cf[4][8];
    {
      const int q = r >> 2, p = r & 3;
      const int base = L_C + (8 * g + 2 * q) * CP + (128 * w + 4 * p) * 2;
#pragma unroll
      for (int t = 0; t < 4; t++)
#pragma unroll
        for (int T = 0; T < 8; T++) cf[t][T] = trfrag(L, base + t * 32 * CP + T * 32, base + t * 32 * CP + CP + T * 32);
    }
    bar();
    bar();
    STAMP(6);
    f32x4 acc[8];
#pragma unroll
    for (int T = 0; T < 8; T++) acc[T] = f32x4{0.f, 0.f, 0.f, 0.f};
#pragma unroll
    for (int t = 0; t < 4; t++) {
      const v4u pf = lds16(L, L_P + r * PP + t * 64 + g * 16);
#pragma unroll
      for (int T = 0; T < 8; T++) acc[T] = mfma(pf, cf[t][T], acc[T]);
    }
#pragma unroll
    for (int k = 0; k < 4; k++) {
      const v4u o = {nosent(pk2(acc[0][k], acc[1][k])), nosent(pk2(acc[2][k], acc[3][k])), nosent(pk2(acc[4][k], acc[5][k])), nosent(pk2(acc[6][k], acc[7][k]))};
      st16(rs, WS_A + (hg * 128 + n) * 16384 + w * 4096 + k * 1024 + lane * 16, o);
    }
    STAMP(7);
  } else {
    const int u = w - 4;
    if (u == 0) tag = ld4(rs, WS_EPOCH) + 1u;
    {
      const char* src = (const char*)ckv + (size_t)(n * 128 + u * 32) * 1024;
      unsigned dst = lbase + L_C + u * 32 * CP;
#pragma unroll 1
      for (int k = 0; k < INFL; k++) { dma16<true>(src, lane * 16, dst); src += 1024; dst += CP; }
#pragma unroll 1
      for (int k = INFL; k < 32; k++) { VMWAIT(INFL - 1); dma16<true>(src, lane * 16, dst); src += 1024; dst += CP; }
    }
    STAMP(1);
    const int prow = 8 * (r >> 3) + (((r & 3) << 1) | ((r >> 2) & 1));
    const int abase = L_C + (32 * u + prow) * CP + g * 256;
    v4u af[2][16];
    VMWAIT(INFL);
#pragma unroll
    for (int s = 0; s < 16; s++) af[0][s] = lds16(L, abase + s * 16);
    if (is_head) {
      const char* vp = (const char*)wuv + (size_t)(128 * q4 + 32 * u + g) * 8192 + hh * 256 + r * 16;
#pragma unroll
      for (int k = 0; k < 8; k++) wv[k] = ldnt(vp + k * 4 * 8192);
      __builtin_amdgcn_sched_barrier(0);
      VMWAIT(8);
    } else {
      VMWAIT(0);
    }
    STAMP(2);
#pragma unroll
    for (int s = 0; s < 16; s++) af[1][s] = lds16(L, abase + 16 * CP + s * 16);
    bar();
    STAMP(3);
    f32x4 d0 = {0.f, 0.f, 0.f, 0.f}, d1 = {0.f, 0.f, 0.f, 0.f};
#pragma unroll
    for (int s = 0; s < 16; s++) {
      const v4u qf = lds16(L, L_Q + r * CP + g * 256 + s * 16);
      d0 = mfma(af[0][s], qf, d0);
      d1 = mfma(af[1][s], qf, d1);
    }
    float sc[8];
#pragma unroll
    for (int k = 0; k < 4; k++) { sc[k] = rbf(rbf(d0[k]) * 0.08838834764831845f); sc[4 + k] = rbf(rbf(d1[k]) * 0.08838834764831845f); }
    float m = fmaxf(fmaxf(fmaxf(sc[0], sc[1]), fmaxf(sc[2], sc[3])), fmaxf(fmaxf(sc[4], sc[5]), fmaxf(sc[6], sc[7])));
    m = mx32(mx16(m));
    if (g == 0) *(LAS float*)(L + L_MX + r * 16 + u * 4) = m;
    STAMP(4);
    bar();
    const f32x4 mm = *(LAS f32x4*)(L + L_MX + r * 16);
    m = fmaxf(fmaxf(mm.x, mm.y), fmaxf(mm.z, mm.w));
    float ls = 0.f;
#pragma unroll
    for (int k = 0; k < 8; k++) { sc[k] = __builtin_amdgcn_exp2f((sc[k] - m) * 1.4426950408889634f); ls += sc[k]; }
    *(LAS v2u*)(L + L_P + r * PP + (32 * u + 4 * g) * 2) = v2u{pk2(sc[0], sc[1]), pk2(sc[2], sc[3])};
    *(LAS v2u*)(L + L_P + r * PP + (32 * u + 16 + 4 * g) * 2) = v2u{pk2(sc[4], sc[5]), pk2(sc[6], sc[7])};
    *(LAS float*)(L + L_LS + r * 64 + (u * 4 + g) * 4) = ls;
    bar();
    STAMP(5);
    if (u == 0) {
      const f32x4 l4 = *(LAS f32x4*)(L + L_LS + r * 64 + g * 16);
      float l = (l4.x + l4.y) + (l4.z + l4.w);
      l = rs16(l, l); l = rs32(l, l);
      if (g == 0) st8(rs, WS_ML + (hg * 128 + n) * 128 + r * 8, v2u{nosent(fu(m)), nosent(fu(l))});
    }
    if (is_head) {
#pragma unroll
      for (int k = 0; k < 8; k++) *(LAS v4u*)(L + L_C + (32 * u + 4 * k + g) * PP + r * 16) = wv[k];
      if (u == 1) {
        v2u p0, p1;
        for (unsigned sp = 0;; sp++) {
          FENCE();
          p0 = ld8(rs, WS_ML + (hg * 128 + lane) * 128 + (hh & 15) * 8);
          p1 = ld8(rs, WS_ML + (hg * 128 + 64 + lane) * 128 + (hh & 15) * 8);
          if (__all(max(max(p0.x, p0.y), max(p1.x, p1.y)) != SENT) || sp > SPIN_CAP) break;
        }
        const float gm = wave_max(fmaxf(uf(p0.x), uf(p1.x)));
        const float e0 = __builtin_amdgcn_exp2f((uf(p0.x) - gm) * 1.4426950408889634f), e1 = __builtin_amdgcn_exp2f((uf(p1.x) - gm) * 1.4426950408889634f);
        const float inv = 1.f / wave_sum(e0 * uf(p0.y) + e1 * uf(p1.y));
        *(LAS float*)(L + L_WG + lane * 4) = e0 * inv;
        *(LAS float*)(L + L_WG + 256 + lane * 4) = e1 * inv;
        STAMP(6);
      }
    } else if (is_wo || is_f1 || is_f2) {
      if (is_f2) {
        const char* src = (const char*)w2 + (size_t)u * 448 * 1024 + (64 * x + 16 * e) * 2;
        unsigned dst = lbase + L_C + u * 14 * 1024;
#pragma unroll 1
        for (int k = 0; k < 14; k++) { dma16<false>(src, (lane >> 1) * 1024 + (lane & 1) * 16, dst); src += 32 * 1024; dst += 1024; }
      } else {
        const int pitch = is_wo ? 1024 : 3584;
        const char* src = is_wo ? (const char*)wo + (size_t)(16 * (u >> 1) + 2 * x + (u & 1)) * 128 * 1024 + cg * 128
                                : (const char*)w1 + (size_t)u * 128 * 3584 + f1 * 128;
        unsigned dst = lbase + L_C + u * 16 * 1024;
#pragma unroll 1
        for (int k = 0; k < 16; k++) { dma16<true>(src, (lane >> 3) * pitch + (lane & 7) * 16, dst); src += 8 * pitch; dst += 1024; }
      }
      if (u == 0) {
        tag = __builtin_amdgcn_readfirstlane(tag);
        for (unsigned sp = 0;; sp++) {
          FENCE();
          const unsigned t0 = ld4(rs, WS_Q2 + (hg ^ 1) * 32768 + lane * 256 + 4), t1 = ld4(rs, WS_Q2 + (hg ^ 1) * 32768 + (64 + lane) * 256 + 4);
          if (__all((t0 == tag) & (t1 == tag)) || sp > SPIN_CAP) break;
        }
      }
      VMWAIT(0);
      STAMP(6);
    }
  }
  if (!(is_head || is_wo || is_f1 || is_f2)) return;
  bar();
  STAMP(8);
  const int q = r >> 2, p = r & 3;
  if (is_head) {
    v4u wf[2][4];
    if (w < 4) {
#pragma unroll
      for (int T = 0; T < 2; T++)
#pragma unroll
        for (int s = 0; s < 4; s++) { const int o = L_C + (16 * q + 4 * s + g) * PP + (32 * w + 16 * T + 4 * p) * 2; wf[T][s] = trfrag(L, o, o + 64 * PP); }
    }
    float wg[4];
#pragma unroll
    for (int k = 0; k < 4; k++) wg[k] = *(LAS float*)(L + L_WG + (16 * w + 4 * k + g) * 4);
    v4u pl[4];
    const int hl = hh & 15;
    for (unsigned sp = 0;; sp++) {
      FENCE();
#pragma unroll
      for (int k = 0; k < 4; k++) pl[k] = ld16(rs, WS_A + (hg * 128 + 16 * w + 4 * k + g) * 16384 + q4 * 4096 + (hl & 3) * 1024 + (hl >> 2) * 256 + r * 16);
      if (__all(max(max(umax4(pl[0]), umax4(pl[1])), max(umax4(pl[2]), umax4(pl[3]))) != SENT) || sp > SPIN_CAP) break;
    }
    STAMP(9);
    float a[8];
#pragma unroll
    for (int t = 0; t < 4; t++) {
      a[2 * t] = wg[0] * bflo(pl[0][t]) + wg[1] * bflo(pl[1][t]) + wg[2] * bflo(pl[2][t]) + wg[3] * bflo(pl[3][t]);
      a[2 * t + 1] = wg[0] * bfhi(pl[0][t]) + wg[1] * bfhi(pl[1][t]) + wg[2] * bfhi(pl[2][t]) + wg[3] * bfhi(pl[3][t]);
    }
    const float c0 = rs32(a[0], a[4]), c1 = rs32(a[1], a[5]), c2 = rs32(a[2], a[6]), c3 = rs32(a[3], a[7]);
    *(LAS v2u*)(L + L_P + (2 * w + (g & 1)) * PP + (8 * r + 4 * (g >> 1)) * 2) = v2u{pk2(c0, c1), pk2(c2, c3)};
    bar();
    if (w < 4) {
      f32x4 d0 = {0.f, 0.f, 0.f, 0.f}, d1 = {0.f, 0.f, 0.f, 0.f};
#pragma unroll
      for (int s = 0; s < 4; s++) {
        const v4u of = lds16(L, L_P + r * PP + s * 64 + g * 16);
        d0 = mfma(of, wf[0][s], d0);
        d1 = mfma(of, wf[1][s], d1);
      }
      float v = rs32((d0.x + d0.y) + (d0.z + d0.w), (d1.x + d1.y) + (d1.z + d1.w));
      v = rs16(v, v);
      if ((g & 1) == 0) st4(rs, WS_A2 + (hh * 4 + q4) * 512 + (32 * w + 16 * (g >> 1) + r) * 4, nosent(fu(v)));
      STAMP(10);
    }
  } else if (is_wo || is_f1) {
    v4u wf[2][4];
#pragma unroll
    for (int s = 0; s < 2; s++)
#pragma unroll
      for (int T = 0; T < 4; T++) { const int o = L_C + (64 * w + 32 * s + 8 * g + q) * 128 + (16 * T + 4 * p) * 2; wf[s][T] = trfrag(L, o, o + 4 * 128); }
    const int col = 4 * r + 2 * (g >> 1) + (g & 1);
    float v;
    if (is_wo) {
      const int hs = w >> 1, head = 16 * (hs >> 1) + 2 * x + (hs & 1);
      v4u pl;
      for (unsigned sp = 0;; sp++) {
        FENCE();
        pl = ld16(rs, WS_A2 + (head * 4 + g) * 512 + (w & 1) * 256 + r * 16);
        if (__all(umax4(pl) != SENT) || sp > SPIN_CAP) break;
      }
      STAMP(9);
      v = rs16(rs32(uf(pl.x), uf(pl.z)), rs32(uf(pl.y), uf(pl.w)));
    } else {
      const float xc = bflo(*(const GAS u16*)(xin + 64 * w + col));
      v4u p0, p1;
      for (unsigned sp = 0;; sp++) {
        FENCE();
        p0 = ld16(rs, WS_B + (w * 8 + g) * 256 + r * 16);
        p1 = ld16(rs, WS_B + (w * 8 + 4 + g) * 256 + r * 16);
        if (__all(max(umax4(p0), umax4(p1)) != SENT) || sp > SPIN_CAP) break;
      }
      STAMP(9);
      v = rs16(rs32(uf(p0.x) + uf(p1.x), uf(p0.z) + uf(p1.z)), rs32(uf(p0.y) + uf(p1.y), uf(p0.w) + uf(p1.w)));
      v = xc + rbf(v);
    }
    *(LAS u16*)(L + L_BN + w * 256 + col * 2) = (u16)pk2(v, v);
    FENCE();
    f32x4 d[4], ds = {0.f, 0.f, 0.f, 0.f};
#pragma unroll
    for (int T = 0; T < 4; T++) d[T] = f32x4{0.f, 0.f, 0.f, 0.f};
#pragma unroll
    for (int s = 0; s < 2; s++) {
      v4u af = lds16(L, L_BN + w * 256 + s * 64 + g * 16);
      if (p == 1) af = v4u{0x3F803F80u, 0x3F803F80u, 0x3F803F80u, 0x3F803F80u};
#pragma unroll
      for (int T = 0; T < 4; T++) d[T] = mfma(af, wf[s][T], d[T]);
      ds = mfma(af, af, ds);
    }
    const f32x4 dz = g == 0 ? d[0] : g == 1 ? d[1] : g == 2 ? d[2] : d[3];
    *(LAS f32x2*)(L + L_PART + (w * 64 + lane) * 8) = f32x2{dz.x, dz.y};
    if (lane == 0) *(LAS f32x2*)(L + L_STAT + w * 8) = f32x2{ds.x, ds.y};
    float bias = 0.f;
    if (is_f1 && w == 0) bias = bflo(*(const GAS u16*)(b1 + 64 * f1 + lane));
    bar();
    if (w == 0) {
      f32x2 t = {0.f, 0.f}, st = {0.f, 0.f};
#pragma unroll
      for (int k = 0; k < 8; k++) { t += *(LAS f32x2*)(L + L_PART + (k * 64 + lane) * 8); st += *(LAS f32x2*)(L + L_STAT + k * 8); }
      if (is_wo) {
        st4(rs, WS_B + (cg * 8 + x) * 256 + lane * 4, nosent(fu(t.x)));
      } else {
        const float mu = st.y * (1.f / 512.f);
        const float var = rbf(fmaxf(st.x * (1.f / 512.f) - mu * mu, 0.f));
        const float den = rbf(sqrtf(rbf(var + 1e-5f)));
        const float uu = rbf(rbf((t.x - rbf(mu) * t.y) / den) + bias);
        const float z = 0.7978845608028654f * (uu + 0.044715f * uu * uu * uu);
        const float ge = uu / (1.f + __builtin_amdgcn_exp2f(-2.885390081777927f * z));
        const float go = dpp<0xB1>(ge);
        if ((lane & 1) == 0) st4(rs, WS_C + f1 * 128 + lane * 2, nosent(pk2(ge, go)));
      }
      STAMP(10);
    }
  } else {
    v4u wf[7];
#pragma unroll
    for (int s = 0; s < 7; s++) { const int o = L_C + (32 * (7 * w + s) + 8 * g + q) * 32 + 8 * p; wf[s] = trfrag(L, o, o + 4 * 32); }
    float hres = 0.f, bias = 0.f;
    if (w == 0) {
      const float xc = bflo(*(const GAS u16*)(xin + 64 * x + 16 * e + r));
      bias = bflo(*(const GAS u16*)(b2 + 64 * x + 16 * e + r));
      unsigned p0, p1;
      for (unsigned sp = 0;; sp++) {
        FENCE();
        p0 = ld4(rs, WS_B + (x * 8 + g) * 256 + (16 * e + r) * 4);
        p1 = ld4(rs, WS_B + (x * 8 + 4 + g) * 256 + (16 * e + r) * 4);
        if (__all(max(p0, p1) != SENT) || sp > SPIN_CAP) break;
      }
      float v = uf(p0) + uf(p1);
      v = rs32(v, v); v = rs16(v, v);
      hres = rbf(xc + rbf(v));
    }
    v4u af;
    const int rr = r < 7 ? r : 6;
    for (unsigned sp = 0;; sp++) {
      FENCE();
      af = ld16(rs, WS_C + (7 * w + rr) * 64 + g * 16);
      if (__all(umax4(af) != SENT) || sp > SPIN_CAP) break;
    }
    STAMP(9);
    f32x4 d[7];
#pragma unroll
    for (int s = 0; s < 7; s++) d[s] = mfma(af, wf[s], f32x4{0.f, 0.f, 0.f, 0.f});
    const float v = g == 0 ? (d[0].x + d[1].y) + (d[2].z + d[3].w) : (d[4].x + d[5].y) + d[6].z;
    if (g < 2) *(LAS float*)(L + L_PART + r * 64 + (2 * w + g) * 4) = v;
    bar();
    if (w == 0) {
      const f32x4 t = *(LAS f32x4*)(L + L_PART + r * 64 + g * 16);
      float yv = (t.x + t.y) + (t.z + t.w);
      yv = rs32(yv, yv); yv = rs16(yv, yv);
      const float o = hres + rbf(rbf(yv) + bias);
      if (g == 0) out[64 * x + 16 * e + r] = (u16)pk2(o, o);
      if (b == 96 + 96 && lane == 0) st4(rs, WS_EPOCH, tag);
      STAMP(10);
    }
  }
}

extern "C" void kernel_launch(void* const* d_in, const int* in_sizes, int n_in, void* d_out, int out_size, void* d_ws, size_t ws_size, hipStream_t stream) {
  (void)hipFuncSetAttribute((const void*)fused_block, hipFuncAttributeMaxDynamicSharedMemorySize, L_BYTES);
  fused_block<<<256, 512, L_BYTES, stream>>>((const u16*)d_in[0], (const u16*)d_in[1], (const u16*)d_in[2], (const u16*)d_in[3], (const u16*)d_in[4],
                                             (const u16*)d_in[5], (const u16*)d_in[7], (const u16*)d_in[8], (const u16*)d_in[9], (const u16*)d_in[10],
                                             (u16*)d_out, (unsigned char*)d_ws);
}
```

```cpp
#include <hip/hip_runtime.h>
#include <hip/hip_bf16.h>

#define GAS __attribute__((address_space(1)))
#define LAS __attribute__((address_space(3)))
#define DI __device__ __forceinline__
typedef unsigned short u16;
typedef unsigned long long u64;
typedef unsigned v4u __attribute__((ext_vector_type(4)));
typedef unsigned v2u __attribute__((ext_vector_type(2)));
typedef float f32x4 __attribute__((ext_vector_type(4)));
typedef float f32x2 __attribute__((ext_vector_type(2)));
typedef short bf16x8 __attribute__((ext_vector_type(8)));
typedef short s16x4 __attribute__((ext_vector_type(4)));
typedef __bf16 bf16x2 __attribute__((ext_vector_type(2)));

constexpr int WS_EPOCH = 0;
constexpr int WS_Q1 = 4096;
constexpr int WS_Q2 = WS_Q1 + 262144;
constexpr int WS_ML = WS_Q2 + 65536;
constexpr int WS_A2 = WS_ML + 65536;
constexpr int WS_B = WS_A2 + 65536;
constexpr int WS_C = WS_B + 16384;
constexpr int WS_A = 1 << 20;
constexpr int WS_BYTES = WS_A + (4 << 20);
constexpr int CP = 1040;
constexpr int L_C = 0;
constexpr int L_Q = 128 * CP;
constexpr int L_P = L_Q + 16 * CP;
constexpr int PP = 272;
constexpr int L_MX = L_P + 16 * PP;
constexpr int L_LS = L_MX + 256;
constexpr int L_BN = L_LS + 1024;
constexpr int L_PART = L_BN + 2048;
constexpr int L_STAT = L_PART + 4096;
constexpr int L_FLAG = L_STAT + 64;
constexpr int L_BYTES = L_FLAG + 64;
static_assert(L_BYTES <= 163840, "LDS");
constexpr unsigned SENT = 0xFFFFFFFFu;
constexpr unsigned SPIN_CAP = 1u << 15;
#define INFL 6
#define PRE 2
constexpr float LOG2E = 1.4426950408889634f;

DI float bflo(unsigned u) { return __builtin_bit_cast(float, u << 16); }
DI float bfhi(unsigned u) { return __builtin_bit_cast(float, u & 0xFFFF0000u); }
DI unsigned pk2(float a, float b) { f32x2 f = {a, b}; return __builtin_bit_cast(unsigned, __builtin_convertvector(f, bf16x2)); }
DI float rbf(float a) { return bflo(pk2(a, a)); }
DI unsigned fu(float a) { return __builtin_bit_cast(unsigned, a); }
DI float uf(unsigned a) { return __builtin_bit_cast(float, a); }
DI float vmax(float a, float b) { return __builtin_elementwise_maximum(a, b); }
DI float rcp(float a) { return __builtin_amdgcn_rcpf(a); }
DI float ex2(float a) { return __builtin_amdgcn_exp2f(a); }
DI unsigned nosent(unsigned a) { return a < 0xFFFFFFFEu ? a : 0xFFFFFFFEu; }
DI f32x4 mfma(v4u a, v4u b, f32x4 c) { return __builtin_amdgcn_mfma_f32_16x16x32_bf16(__builtin_bit_cast(bf16x8, a), __builtin_bit_cast(bf16x8, b), c, 0, 0, 0); }
DI float dot2(unsigned a, unsigned b, float c) { return __builtin_amdgcn_fdot2_f32_bf16(__builtin_bit_cast(bf16x2, a), __builtin_bit_cast(bf16x2, b), c, false); }
DI float rs32(float a, float b) { auto r = __builtin_amdgcn_permlane32_swap(fu(a), fu(b), false, false); return uf(r[0]) + uf(r[1]); }
DI float rs16(float a, float b) { auto r = __builtin_amdgcn_permlane16_swap(fu(a), fu(b), false, false); return uf(r[0]) + uf(r[1]); }
DI float mx32(float a) { auto r = __builtin_amdgcn_permlane32_swap(fu(a), fu(a), false, false); return vmax(uf(r[0]), uf(r[1])); }
DI float mx16(float a) { auto r = __builtin_amdgcn_permlane16_swap(fu(a), fu(a), false, false); return vmax(uf(r[0]), uf(r[1])); }
template <int CTRL> DI float dpp(float a) { return uf((unsigned)__builtin_amdgcn_update_dpp(0, (int)fu(a), CTRL, 0xF, 0xF, true)); }
DI float row_sum(float a) { a += dpp<0x128>(a); a += dpp<0x124>(a); a += dpp<0x122>(a); a += dpp<0x121>(a); return a; }
DI v4u ldnt(const void* p) { return __builtin_nontemporal_load((const GAS v4u*)p); }
DI v4u ld16(__amdgpu_buffer_rsrc_t rs, int off) { return __builtin_amdgcn_raw_buffer_load_b128(rs, off, 0, 16); }
DI unsigned ld4(__amdgpu_buffer_rsrc_t rs, int off) { return __builtin_amdgcn_raw_buffer_load_b32(rs, off, 0, 16); }
DI void st16(__amdgpu_buffer_rsrc_t rs, int off, v4u v) { __builtin_amdgcn_raw_buffer_store_b128(v, rs, off, 0, 16); }
DI void st4(__amdgpu_buffer_rsrc_t rs, int off, unsigned v) { __builtin_amdgcn_raw_buffer_store_b32(v, rs, off, 0, 16); }
DI unsigned umax4(v4u v) { return max(max(v.x, v.y), max(v.z, v.w)); }
DI void bar() { asm volatile("s_waitcnt lgkmcnt(0)\n\ts_barrier" ::: "memory"); }
#define FENCE() asm volatile("" ::: "memory")
#define SCHED() __builtin_amdgcn_sched_barrier(0)
#define VMWAIT(N) asm volatile("s_waitcnt vmcnt(%0)" ::"i"(N) : "memory")
template <bool NT> DI void dma16(const void* base, unsigned voff, unsigned lds) {
  unsigned keep;
  if (NT) asm volatile("s_mov_b32 %0, m0\n\ts_mov_b32 m0, %3\n\ts_nop 0\n\tglobal_load_lds_dwordx4 %1, %2 nt\n\ts_mov_b32 m0, %0" : "=&s"(keep) : "v"(voff), "s"(base), "s"(lds) : "memory");
  else asm volatile("s_mov_b32 %0, m0\n\ts_mov_b32 m0, %3\n\ts_nop 0\n\tglobal_load_lds_dwordx4 %1, %2\n\ts_mov_b32 m0, %0" : "=&s"(keep) : "v"(voff), "s"(base), "s"(lds) : "memory");
}
DI v4u lds16(LAS unsigned char* L, int off) { return *(LAS v4u*)(L + off); }
DI v4u trfrag(LAS unsigned char* L, int off0, int off1) {
  s16x4 a = __builtin_amdgcn_ds_read_tr16_b64_v4i16((LAS s16x4*)(L + off0));
  s16x4 b = __builtin_amdgcn_ds_read_tr16_b64_v4i16((LAS s16x4*)(L + off1));
  v2u x = __builtin_bit_cast(v2u, a), y = __builtin_bit_cast(v2u, b);
  return v4u{x.x, x.y, y.x, y.y};
}
#define STAMP(k)
#define STAMP_DUMP()

__global__ void __launch_bounds__(512, 2)
fused_block(const u16* __restrict__ wq, const u16* __restrict__ wuk, const u16* __restrict__ xin, unsigned char* ws,
            const u16* __restrict__ ckv, const u16* __restrict__ wuv, const u16* __restrict__ wo, const u16* __restrict__ w1,
            const u16* __restrict__ b1, const u16* __restrict__ w2, const u16* __restrict__ b2, u16* __restrict__ out) {
  extern __shared__ __attribute__((aligned(16))) unsigned char smem[];
  LAS unsigned char* L = (LAS unsigned char*)smem;
  const unsigned lbase = (unsigned)(size_t)L;
  const int b = blockIdx.x, lane = threadIdx.x & 63, w = __builtin_amdgcn_readfirstlane(threadIdx.x >> 6);
  const int x = b & 7, hg = (b >> 3) & 1, i = b >> 4;
  const int g = lane >> 4, r = lane & 15;
  const int h = 16 * hg + 2 * x + (i >> 3), j = i & 7;
  STAMP(0);
  asm volatile("" :: "s"(wq), "s"(wuk), "s"(xin), "s"(ws), "s"(ckv), "s"(wuv), "s"(wo), "s"(w1), "s"(b1), "s"(w2), "s"(b2), "s"(out));
  v4u wa[4], xa; v2u xr; unsigned tag;
  if (w < 4) {
    const char* base = (const char*)wq + (size_t)j * (64 * 8192) + h * 256;
    const unsigned off = (lane >> 2) * (4 * 8192) + w * 64 + (lane & 3) * 16;
    asm volatile("global_load_dwordx4 %4, %12, %14\n\tglobal_load_dwordx2 %5, %13, %14\n\t"
                 "global_load_dwordx4 %0, %7, %11 nt\n\tglobal_load_dwordx4 %1, %8, %11 nt\n\tglobal_load_dwordx4 %2, %9, %11 nt\n\tglobal_load_dwordx4 %3, %10, %11 nt\n\t"
                 "global_load_dword %6, %15, %16 sc1"
                 : "=&v"(wa[0]), "=&v"(wa[1]), "=&v"(wa[2]), "=&v"(wa[3]), "=&v"(xa), "=&v"(xr), "=&v"(tag)
                 : "v"(off), "v"(off + 8192), "v"(off + 2 * 8192), "v"(off + 3 * 8192), "s"(base),
                   "v"(lane * 16), "v"((64 * j + 4 * (lane >> 2)) * 2), "s"(xin), "v"(WS_EPOCH), "s"(ws) : "memory");
  }
  if (threadIdx.x == 256) *(volatile LAS v2u*)(L + L_FLAG) = v2u{0u, 0u};
  bar();
  u64 pc0; int clen;
  asm volatile("s_getpc_b64 %0\n\t.Lpf_here%=:\n\ts_mov_b32 %1, .Lfb_code_end-.Lpf_here%=" : "=s"(pc0), "=s"(clen));
  const __amdgpu_buffer_rsrc_t rs = __builtin_amdgcn_make_buffer_rsrc(ws, 0, WS_BYTES, 0x00020000);
  const int n = i * 8 + x;
  const char* src = (const char*)ckv + (size_t)(n * 128 + w * 32) * 1024;
  unsigned dst = lbase + L_C + w * 32 * CP;
  int w0_ = w, w1_ = w, w2_ = w;
  asm volatile("" : "+s"(w0_));
  if (w0_ >= 4) {
    const int v = w - 4;
    const char* base = (const char*)wuk + (size_t)(64 * j + 16 * v) * 8192 + h * 256;
    unsigned keep;
    const int off = max(min((v * 32 + (lane & 31)) * 128, clen - 4), 0);
    asm volatile("s_mov_b32 %0, m0\n\ts_mov_b32 m0, %3\n\ts_nop 0\n\tglobal_load_lds_dword %1, %2\n\ts_mov_b32 m0, %0" : "=&s"(keep) : "v"(off), "s"(pc0), "s"(lbase + L_PART + v * 256) : "memory");
#pragma unroll
    for (int k = 0; k < 4; k++) dma16<true>(base, (4 * k + g) * 8192 + ((r ^ (4 * k + g)) * 16), lbase + L_Q + v * (4 * CP) + k * 1024);
  }
  asm volatile("" : "+s"(w1_));
  if (w1_ < 4) {
#pragma unroll
    for (int k = 0; k < PRE; k++) { dma16<true>(src, lane * 16, dst); src += 1024; dst += CP; }
    STAMP(12);
    asm volatile("s_waitcnt vmcnt(%2)" : "+v"(xa), "+v"(xr) : "i"(PRE + 5) : "memory");
    STAMP(13);
    float xv[8] = {bflo(xa.x), bfhi(xa.x), bflo(xa.y), bfhi(xa.y), bflo(xa.z), bfhi(xa.z), bflo(xa.w), bfhi(xa.w)};
    const float x0 = uf(__builtin_amdgcn_readfirstlane(fu(xv[0])));
#pragma unroll
    for (int k = 0; k < 8; k++) xv[k] -= x0;
    const float s1 = ((xv[0] + xv[1]) + (xv[2] + xv[3])) + ((xv[4] + xv[5]) + (xv[6] + xv[7]));
    const float s2 = (xv[0] * xv[0] + xv[1] * xv[1] + xv[2] * xv[2] + xv[3] * xv[3]) + (xv[4] * xv[4] + xv[5] * xv[5] + xv[6] * xv[6] + xv[7] * xv[7]);
    float ss = rs32(s1, s2);
    ss = rs16(ss, ss);
    ss = row_sum(ss);
    const float md = uf(__builtin_amdgcn_readlane(fu(ss), 0)) * (1.f / 512.f);
    const float m2 = uf(__builtin_amdgcn_readlane(fu(ss), 32)) * (1.f / 512.f);
    const float mean = rbf(x0 + md);
    const float var = rbf(vmax(m2 - md * md, 0.f));
    const float rden = rcp(rbf(__builtin_amdgcn_sqrtf(rbf(var + 1e-5f))));
    const unsigned xm01 = pk2(bflo(xr.x) - mean, bfhi(xr.x) - mean), xm23 = pk2(bflo(xr.y) - mean, bfhi(xr.y) - mean);
    unsigned xn01 = pk2(bflo(xm01) * rden, bfhi(xm01) * rden);
    unsigned xn23 = pk2(bflo(xm23) * rden, bfhi(xm23) * rden);
    int qo = WS_Q1 + (h * 8 + j) * 1024 + (16 * w + 4 * (lane & 3) + g) * 16;
    asm volatile("s_waitcnt vmcnt(%8)" : "+v"(wa[0]), "+v"(wa[1]), "+v"(wa[2]), "+v"(wa[3]), "+v"(tag), "+v"(xn01), "+v"(xn23), "+v"(qo) : "i"(PRE) : "memory");
    STAMP(14);
    tag = __builtin_amdgcn_readfirstlane(tag) + 1u;
    float a[8];
#pragma unroll
    for (int k = 0; k < 4; k++) {
      a[2 * k] = dot2(__builtin_amdgcn_perm(wa[1][k], wa[0][k], 0x05040100), xn01, dot2(__builtin_amdgcn_perm(wa[3][k], wa[2][k], 0x05040100), xn23, 0.f));
      a[2 * k + 1] = dot2(__builtin_amdgcn_perm(wa[1][k], wa[0][k], 0x07060302), xn01, dot2(__builtin_amdgcn_perm(wa[3][k], wa[2][k], 0x07060302), xn23, 0.f));
    }
    const float c0 = rs32(a[0], a[4]), c1 = rs32(a[1], a[5]), c2 = rs32(a[2], a[6]), c3 = rs32(a[3], a[7]);
    float d0 = rs16(c0, c2), d1 = rs16(c1, c3);
    d0 += dpp<0x128>(d0); d1 += dpp<0x128>(d1);
    d0 += dpp<0x124>(d0); d1 += dpp<0x124>(d1);
    if ((lane & 12) == 0) st16(rs, qo, v4u{fu(d0), tag, fu(d1), tag});
  } else {
    const int v = w - 4;
    tag = ld4(rs, WS_EPOCH);
    STAMP(12);
    VMWAIT(0);
    tag = __builtin_amdgcn_readfirstlane(tag) + 1u;
    v4u kf[4];
#pragma unroll
    for (int s = 0; s < 4; s++) kf[s] = lds16(L, L_Q + v * (4 * CP) + r * 256 + (((4 * s + g) ^ r) * 16));
    STAMP(1);
    if (v == 0) {
      float q0, q1;
      for (unsigned sp = 0;; sp++) {
        FENCE();
        v4u p[8];
#pragma unroll
        for (int k = 0; k < 8; k++) p[k] = ld16(rs, WS_Q1 + (h * 8 + k) * 1024 + lane * 16);
        unsigned bad = 0;
#pragma unroll
        for (int k = 0; k < 8; k++) bad |= (p[k].y ^ tag) | (p[k].w ^ tag);
        q0 = ((uf(p[0].x) + uf(p[1].x)) + (uf(p[2].x) + uf(p[3].x))) + ((uf(p[4].x) + uf(p[5].x)) + (uf(p[6].x) + uf(p[7].x)));
        q1 = ((uf(p[0].z) + uf(p[1].z)) + (uf(p[2].z) + uf(p[3].z))) + ((uf(p[4].z) + uf(p[5].z)) + (uf(p[6].z) + uf(p[7].z)));
        if (__all(bad == 0) || sp > SPIN_CAP) break;
      }
      *(volatile LAS unsigned*)(L + L_BN + lane * 4) = pk2(q0, q1);
      *(volatile LAS unsigned*)(L + L_FLAG) = 1u;
      STAMP(2);
    }
    v4u qf[4];
    for (unsigned sp = 0;; sp++) {
      const unsigned f = *(volatile LAS unsigned*)(L + L_FLAG);
#pragma unroll
      for (int s = 0; s < 4; s++) qf[s] = *(volatile LAS v4u*)(L + L_BN + s * 64 + g * 16);
      if (__builtin_amdgcn_readfirstlane(f) != 0u || sp > (SPIN_CAP << 4)) break;
    }
    f32x4 o = {0.f, 0.f, 0.f, 0.f};
#pragma unroll
    for (int s = 0; s < 4; s++) o = mfma(kf[s], qf[s], o);
    if (r == 0) st16(rs, WS_Q2 + (h * 8 + j) * 256 + (4 * v + g) * 16, v4u{pk2(o.x, o.y), tag, pk2(o.z, o.w), tag});
    FENCE();
    if (v == 3) *(volatile LAS unsigned*)(L + L_FLAG + 4) = 1u;
  }
  const bool is_head = i < 8, is_wo = (i >> 2) == 2;
  const int e = ((i - 12) << 1) | hg;
  const bool is_f2 = i >= 12 && e < 4;
  const int f1 = (e - 4) * 8 + x;
  const bool is_f1 = i >= 12 && e >= 4 && f1 < 28;
  const int hh = 16 * hg + 2 * x + (i >> 2), q4 = i & 3;
  const int cg = ((i - 8) << 1) | hg;
  asm volatile("" : "+s"(w2_));
  if (w2_ < 4) {
    STAMP(1);
    {
      int k = PRE;
#pragma unroll 1
      for (; k < INFL; k++) { dma16<true>(src, lane * 16, dst); src += 1024; dst += CP; }
#pragma unroll 1
      for (; k < 32; k++) {
        if (__builtin_amdgcn_readfirstlane(*(volatile LAS unsigned*)(L + L_FLAG + 4)) != 0u) break;
        VMWAIT(INFL - 1);
        dma16<true>(src, lane * 16, dst); src += 1024; dst += CP;
      }
#pragma unroll 1
      for (; k < 32; k++) { dma16<true>(src, lane * 16, dst); src += 1024; dst += CP; }
    }
    STAMP(2);
    const int prow = 8 * (r >> 3) + (((r & 3) << 1) | ((r >> 2) & 1));
    const int abase = L_C + (32 * w + prow) * CP + g * 256;
    v4u af[2][16];
    v4u wv[8];
    if (is_head) {
      const char* vp = (const char*)wuv + (size_t)(128 * q4 + 32 * w + g) * 8192 + hh * 256 + r * 16;
#pragma unroll
      for (int k = 0; k < 8; k++) wv[k] = ldnt(vp + k * 4 * 8192);
      SCHED();
      VMWAIT(24);
    } else {
      VMWAIT(16);
    }
#pragma unroll
    for (int s = 0; s < 16; s++) af[0][s] = lds16(L, abase + s * 16);
    if (is_head) { VMWAIT(8); } else { VMWAIT(0); }
    STAMP(3);
#pragma unroll
    for (int s = 0; s < 16; s++) af[1][s] = lds16(L, abase + 16 * CP + s * 16);
    bar();
    STAMP(4);
    f32x4 d0 = {0.f, 0.f, 0.f, 0.f}, d1 = {0.f, 0.f, 0.f, 0.f};
    v4u qf[16];
#pragma unroll
    for (int s = 0; s < 16; s++) qf[s] = lds16(L, L_Q + r * CP + g * 256 + s * 16);
    SCHED();
#pragma unroll
    for (int s = 0; s < 16; s++) {
      d0 = mfma(af[0][s], qf[s], d0);
      d1 = mfma(af[1][s], qf[s], d1);
    }
    float sc[8];
    {
      const float c = 0.08838834764831845f;
      const unsigned r0 = pk2(d0.x, d0.y), r1 = pk2(d0.z, d0.w), r2 = pk2(d1.x, d1.y), r3 = pk2(d1.z, d1.w);
      const unsigned t0 = pk2(bflo(r0) * c, bfhi(r0) * c), t1 = pk2(bflo(r1) * c, bfhi(r1) * c), t2 = pk2(bflo(r2) * c, bfhi(r2) * c), t3 = pk2(bflo(r3) * c, bfhi(r3) * c);
      sc[0] = bflo(t0); sc[1] = bfhi(t0); sc[2] = bflo(t1); sc[3] = bfhi(t1); sc[4] = bflo(t2); sc[5] = bfhi(t2); sc[6] = bflo(t3); sc[7] = bfhi(t3);
    }
    float m = vmax(vmax(vmax(vmax(sc[0], sc[1]), sc[2]), vmax(vmax(sc[3], sc[4]), sc[5])), vmax(sc[6], sc[7]));
    m = mx32(mx16(m));
    *(LAS float*)(L + L_MX + r * 16 + w * 4) = m;
    STAMP(5);
    bar();
    const f32x4 mm = *(LAS f32x4*)(L + L_MX + r * 16);
    m = vmax(vmax(vmax(mm.x, mm.y), mm.z), mm.w);
    const float ml = m * LOG2E;
#pragma unroll
    for (int k = 0; k < 8; k++) sc[k] = ex2(sc[k] * LOG2E - ml);
    *(LAS v2u*)(L + L_P + r * PP + (32 * w + 4 * g) * 2) = v2u{pk2(sc[0], sc[1]), pk2(sc[2], sc[3])};
    *(LAS v2u*)(L + L_P + r * PP + (32 * w + 16 + 4 * g) * 2) = v2u{pk2(sc[4], sc[5]), pk2(sc[6], sc[7])};
    *(LAS float*)(L + L_LS + r * 64 + (w * 4 + g) * 4) = ((sc[0] + sc[1]) + (sc[2] + sc[3])) + ((sc[4] + sc[5]) + (sc[6] + sc[7]));
    bar();
    STAMP(6);
    if (w == 0) {
      const f32x4 l4 = *(LAS f32x4*)(L + L_LS + r * 64 + g * 16);
      float l = (l4.x + l4.y) + (l4.z + l4.w);
      l = rs16(l, l); l = rs32(l, l);
      if (g == 0) st16(rs, WS_ML + (hg * 128 + n) * 256 + r * 16, v4u{fu(ml), tag, fu(l), tag});
    }
    if (is_head) {
#pragma unroll
      for (int k = 0; k < 8; k++) *(LAS v4u*)(L + L_C + (32 * w + 4 * k + g) * PP + r * 16) = wv[k];
    }
    STAMP(7);
  } else {
    const int v = w - 4;
    STAMP(3);
    {
      const v4u sv = {SENT, SENT, SENT, SENT};
#pragma unroll
      for (int k = 0; k < 4; k++) st16(rs, WS_A + (hg * 128 + n) * 16384 + (v * 4 + k) * 1024 + lane * 16, sv);
      if (v == 0) {
        if (is_head) { if (lane < 32) st16(rs, WS_A2 + (hh * 4 + q4) * 512 + lane * 16, sv); }
        else if (is_wo) { if (lane < 16) st16(rs, WS_B + (cg * 8 + x) * 256 + lane * 16, sv); }
        else if (is_f1) { if (lane < 8) st16(rs, WS_C + f1 * 128 + lane * 16, sv); }
      }
      VMWAIT(0);
    }
    STAMP(4);
    for (unsigned sp = 0;; sp++) {
      FENCE();
      v4u p[8];
#pragma unroll
      for (int k = 0; k < 8; k++) p[k] = ld16(rs, WS_Q2 + hg * 32768 + (v * 8 + k) * 1024 + lane * 16);
      unsigned bad = 0;
#pragma unroll
      for (int k = 0; k < 8; k++) {
        bad |= (p[k].y ^ tag) | (p[k].w ^ tag);
        *(LAS v2u*)(L + L_Q + (4 * v + (k >> 1)) * CP + (k & 1) * 512 + lane * 8) = v2u{p[k].x, p[k].z};
      }
      if (__all(bad == 0) || sp > SPIN_CAP) break;
    }
    STAMP(5);
    bar();
    __builtin_amdgcn_s_sleep(3);
    v4u cf[4][8];
    {
      const int base = L_C + (8 * g + 2 * (r >> 2)) * CP + (128 * v + 4 * (r & 3)) * 2;
#pragma unroll
      for (int t = 0; t < 4; t++)
#pragma unroll
        for (int T = 0; T < 8; T++) cf[t][T] = trfrag(L, base + t * 32 * CP + T * 32, base + t * 32 * CP + CP + T * 32);
    }
    bar();
    bar();
    STAMP(6);
    f32x4 acc[8];
#pragma unroll
    for (int T = 0; T < 8; T++) acc[T] = f32x4{0.f, 0.f, 0.f, 0.f};
    v4u pf[4];
#pragma unroll
    for (int t = 0; t < 4; t++) pf[t] = lds16(L, L_P + r * PP + t * 64 + g * 16);
    SCHED();
#pragma unroll
    for (int t = 0; t < 4; t++)
#pragma unroll
      for (int T = 0; T < 8; T++) acc[T] = mfma(pf[t], cf[t][T], acc[T]);
#pragma unroll
    for (int k = 0; k < 4; k++) {
      const v4u o = {nosent(pk2(acc[0][k], acc[1][k])), nosent(pk2(acc[2][k], acc[3][k])), nosent(pk2(acc[4][k], acc[5][k])), nosent(pk2(acc[6][k], acc[7][k]))};
      st16(rs, WS_A + (hg * 128 + n) * 16384 + v * 4096 + k * 1024 + lane * 16, o);
    }
    STAMP(7);
  }
  if (!(is_head || is_wo || is_f1 || is_f2)) { STAMP_DUMP(); return; }
  bar();
  STAMP(8);
  if (!is_head) {
    if (is_f2) {
      const char* s2 = (const char*)w2 + (size_t)w * 224 * 1024 + (64 * x + 16 * e) * 2;
      unsigned d2 = lbase + L_C + w * 7 * 1024;
#pragma unroll 1
      for (int k = 0; k < 7; k++) { dma16<false>(s2, (lane >> 1) * 1024 + (lane & 1) * 16, d2); s2 += 32 * 1024; d2 += 1024; }
    } else {
      const int pitch = is_wo ? 1024 : 3584;
      const char* s2 = is_wo ? (const char*)wo + ((size_t)(16 * (w >> 2) + 2 * x + ((w >> 1) & 1)) * 128 + (w & 1) * 64) * 1024 + cg * 128
                             : (const char*)w1 + (size_t)w * 64 * 3584 + f1 * 128;
      unsigned d2 = lbase + L_C + w * 8 * 1024;
#pragma unroll 1
      for (int k = 0; k < 8; k++) { dma16<true>(s2, (lane >> 3) * pitch + (lane & 7) * 16, d2); s2 += 8 * pitch; d2 += 1024; }
    }
    VMWAIT(0);
    STAMP(11);
    for (unsigned sp = 0;; sp++) {
      FENCE();
      unsigned bad = 0;
#pragma unroll
      for (int k = 0; k < 4; k++) bad |= ld4(rs, WS_ML + (k * 64 + lane) * 256 + 4) ^ tag;
      if (__all(bad == 0) || sp > SPIN_CAP) break;
    }
  }
  const int q = r >> 2, p = r & 3;
  if (is_head) {
    v4u wf[2][4];
    if (w < 4) {
#pragma unroll
      for (int T = 0; T < 2; T++)
#pragma unroll
        for (int s = 0; s < 4; s++) { const int o = L_C + (16 * q + 4 * s + g) * PP + (32 * w + 16 * T + 4 * p) * 2; wf[T][s] = trfrag(L, o, o + 64 * PP); }
    }
    const int hl = hh & 15;
    v4u ml;
    for (unsigned sp = 0;; sp++) {
      FENCE();
      ml = ld16(rs, WS_ML + (hg * 128 + 16 * w + 4 * p + g) * 256 + hl * 16);
      if (__all(((ml.y ^ tag) | (ml.w ^ tag)) == 0) || sp > SPIN_CAP) break;
    }
    STAMP(11);
    v4u pl[4];
    const int po = WS_A + (hg * 128 + 16 * w + g) * 16384 + q4 * 4096 + (hl & 3) * 1024 + (hl >> 2) * 256 + r * 16;
#pragma unroll
    for (int k = 0; k < 4; k++) pl[k] = ld16(rs, po + k * 65536);
    SCHED();
    float mw = uf(ml.x);
    mw = vmax(mw, dpp<0xB1>(mw)); mw = vmax(mw, dpp<0x4E>(mw));
    mw = mx32(mx16(mw));
    const float ew = ex2(uf(ml.x) - mw);
    float lw = ew * uf(ml.z);
    lw += dpp<0xB1>(lw); lw += dpp<0x4E>(lw);
    lw = rs16(lw, lw); lw = rs32(lw, lw);
    if (lane == 0) *(LAS f32x2*)(L + L_STAT + w * 8) = f32x2{mw, lw};
    const float wg0 = dpp<0x00>(ew), wg1 = dpp<0x55>(ew), wg2 = dpp<0xAA>(ew), wg3 = dpp<0xFF>(ew);
    SCHED();
    for (unsigned sp = 0; !__all(max(max(umax4(pl[0]), umax4(pl[1])), max(umax4(pl[2]), umax4(pl[3]))) != SENT) && sp < SPIN_CAP; sp++) {
      FENCE();
#pragma unroll
      for (int k = 0; k < 4; k++) pl[k] = ld16(rs, po + k * 65536);
    }
    STAMP(9);
    float a[8];
#pragma unroll
    for (int t = 0; t < 4; t++) {
      a[2 * t] = wg0 * bflo(pl[0][t]) + wg1 * bflo(pl[1][t]) + wg2 * bflo(pl[2][t]) + wg3 * bflo(pl[3][t]);
      a[2 * t + 1] = wg0 * bfhi(pl[0][t]) + wg1 * bfhi(pl[1][t]) + wg2 * bfhi(pl[2][t]) + wg3 * bfhi(pl[3][t]);
    }
    const float c0 = rs32(a[0], a[4]), c1 = rs32(a[1], a[5]), c2 = rs32(a[2], a[6]), c3 = rs32(a[3], a[7]);
    *(LAS v2u*)(L + L_P + (2 * w + (g & 1)) * PP + (8 * r + 4 * (g >> 1)) * 2) = v2u{pk2(c0, c1), pk2(c2, c3)};
    bar();
    if (w < 4) {
      v4u of[4];
#pragma unroll
      for (int s = 0; s < 4; s++) of[s] = lds16(L, L_P + r * PP + s * 64 + g * 16);
      const f32x4 st = *(LAS f32x4*)(L + L_STAT + g * 16);
      SCHED();
      f32x4 d0 = {0.f, 0.f, 0.f, 0.f}, d1 = {0.f, 0.f, 0.f, 0.f};
#pragma unroll
      for (int s = 0; s < 4; s++) {
        d0 = mfma(of[s], wf[0][s], d0);
        d1 = mfma(of[s], wf[1][s], d1);
      }
      const float gm = mx32(mx16(vmax(st.x, st.z)));
      const float e0 = ex2(st.x - gm), e1 = ex2(st.z - gm);
      float lt = e0 * st.y + e1 * st.w;
      lt = rs16(lt, lt); lt = rs32(lt, lt);
      float vv = rs32(e0 * (d0.x + d0.y) + e1 * (d0.z + d0.w), e0 * (d1.x + d1.y) + e1 * (d1.z + d1.w));
      vv = rs16(vv, vv) * rcp(lt);
      if ((g & 1) == 0) st4(rs, WS_A2 + (hh * 4 + q4) * 512 + (32 * w + 16 * (g >> 1) + r) * 4, nosent(fu(vv)));
      STAMP(10);
    }
  } else if (is_wo || is_f1) {
    const int col = 4 * r + 2 * (g >> 1) + (g & 1);
    const float xc = bflo(*(const GAS u16*)(xin + 64 * w + col));
    const float bias = bflo(*(const GAS u16*)(b1 + (is_f1 ? 64 * f1 : 0) + lane));
    v4u wf[2][4];
#pragma unroll
    for (int s = 0; s < 2; s++)
#pragma unroll
      for (int T = 0; T < 4; T++) { const int o = L_C + (64 * w + 32 * s + 8 * g + q) * 128 + (16 * T + 4 * p) * 2; wf[s][T] = trfrag(L, o, o + 4 * 128); }
    float v;
    if (is_wo) {
      const int hs = w >> 1, head = 16 * (hs >> 1) + 2 * x + (hs & 1);
      v4u pl;
      for (unsigned sp = 0;; sp++) {
        FENCE();
        pl = ld16(rs, WS_A2 + (head * 4 + g) * 512 + (w & 1) * 256 + r * 16);
        if (__all(umax4(pl) != SENT) || sp > SPIN_CAP) break;
      }
      STAMP(9);
      v = rs16(rs32(uf(pl.x), uf(pl.z)), rs32(uf(pl.y), uf(pl.w)));
    } else {
      v4u p0, p1;
      for (unsigned sp = 0;; sp++) {
        FENCE();
        p0 = ld16(rs, WS_B + (w * 8 + g) * 256 + r * 16);
        p1 = ld16(rs, WS_B + (w * 8 + 4 + g) * 256 + r * 16);
        if (__all(max(umax4(p0), umax4(p1)) != SENT) || sp > SPIN_CAP) break;
      }
      STAMP(9);
      v = rs16(rs32(uf(p0.x) + uf(p1.x), uf(p0.z) + uf(p1.z)), rs32(uf(p0.y) + uf(p1.y), uf(p0.w) + uf(p1.w)));
      v = xc + rbf(v);
    }
    *(LAS u16*)(L + L_BN + w * 256 + col * 2) = (u16)pk2(v, v);
    FENCE();
    v4u af[2];
#pragma unroll
    for (int s = 0; s < 2; s++) af[s] = lds16(L, L_BN + w * 256 + s * 64 + g * 16);
    SCHED();
    f32x4 d[4], ds = {0.f, 0.f, 0.f, 0.f};
#pragma unroll
    for (int T = 0; T < 4; T++) d[T] = f32x4{0.f, 0.f, 0.f, 0.f};
#pragma unroll
    for (int s = 0; s < 2; s++) {
      if (p == 1) af[s] = v4u{0x3F803F80u, 0x3F803F80u, 0x3F803F80u, 0x3F803F80u};
#pragma unroll
      for (int T = 0; T < 4; T++) d[T] = mfma(af[s], wf[s][T], d[T]);
      ds = mfma(af[s], af[s], ds);
    }
    const f32x4 dz = g == 0 ? d[0] : g == 1 ? d[1] : g == 2 ? d[2] : d[3];
    *(LAS f32x2*)(L + L_PART + (w * 64 + lane) * 8) = f32x2{dz.x, dz.y};
    if (lane == 0) *(LAS f32x2*)(L + L_STAT + w * 8) = f32x2{ds.x, ds.y};
    bar();
    if (w == 0) {
      f32x2 tp[8]; f32x4 sq[4];
#pragma unroll
      for (int k = 0; k < 8; k++) tp[k] = *(LAS f32x2*)(L + L_PART + (k * 64 + lane) * 8);
#pragma unroll
      for (int k = 0; k < 4; k++) sq[k] = *(LAS f32x4*)(L + L_STAT + k * 16);
      SCHED();
      const f32x2 t = ((tp[0] + tp[1]) + (tp[2] + tp[3])) + ((tp[4] + tp[5]) + (tp[6] + tp[7]));
      if (is_wo) {
        st4(rs, WS_B + (cg * 8 + x) * 256 + lane * 4, nosent(fu(t.x)));
      } else {
        const f32x4 s4 = (sq[0] + sq[1]) + (sq[2] + sq[3]);
        const float mu = (s4.y + s4.w) * (1.f / 512.f);
        const float var = rbf(vmax((s4.x + s4.z) * (1.f / 512.f) - mu * mu, 0.f));
        const float rden = rcp(rbf(__builtin_amdgcn_sqrtf(rbf(var + 1e-5f))));
        const float uu = rbf(rbf((t.x - rbf(mu) * t.y) * rden) + bias);
        const float z = uu * (0.044715f * uu * uu + 1.f);
        const float ge = uu * rcp(1.f + ex2(-2.f * 0.7978845608028654f * LOG2E * z));
        const float go = dpp<0xB1>(ge);
        if ((lane & 1) == 0) st4(rs, WS_C + f1 * 128 + lane * 2, nosent(pk2(ge, go)));
      }
      STAMP(10);
    }
  } else {
    v4u wf[7];
#pragma unroll
    for (int s = 0; s < 7; s++) { const int o = L_C + (32 * (7 * w + s) + 8 * g + q) * 32 + 8 * p; wf[s] = trfrag(L, o, o + 4 * 32); }
    float hres = 0.f, bias = 0.f;
    {
      const float xc = bflo(*(const GAS u16*)(xin + 64 * x + 16 * e + r));
      bias = bflo(*(const GAS u16*)(b2 + 64 * x + 16 * e + r));
      unsigned p0, p1;
      for (unsigned sp = 0;; sp++) {
        FENCE();
        p0 = ld4(rs, WS_B + (x * 8 + g) * 256 + (16 * e + r) * 4);
        p1 = ld4(rs, WS_B + (x * 8 + 4 + g) * 256 + (16 * e + r) * 4);
        if (__all(max(p0, p1) != SENT) || sp > SPIN_CAP) break;
      }
      float v = uf(p0) + uf(p1);
      v = rs32(v, v); v = rs16(v, v);
      hres = rbf(xc + rbf(v));
    }
    v4u af;
    const int rr = r < 7 ? r : 6;
    for (unsigned sp = 0;; sp++) {
      FENCE();
      af = ld16(rs, WS_C + (7 * w + rr) * 64 + g * 16);
      if (__all(umax4(af) != SENT) || sp > SPIN_CAP) break;
    }
    STAMP(9);
    f32x4 d[7];
#pragma unroll
    for (int s = 0; s < 7; s++) d[s] = mfma(af, wf[s], f32x4{0.f, 0.f, 0.f, 0.f});
    const float v = g == 0 ? (d[0].x + d[1].y) + (d[2].z + d[3].w) : (d[4].x + d[5].y) + d[6].z;
    if (g < 2) *(LAS float*)(L + L_PART + r * 64 + (2 * w + g) * 4) = v;
    bar();
    if (w == 0) {
      const f32x4 t = *(LAS f32x4*)(L + L_PART + r * 64 + g * 16);
      float yv = (t.x + t.y) + (t.z + t.w);
      yv = rs32(yv, yv); yv = rs16(yv, yv);
      const float o = hres + rbf(rbf(yv) + bias);
      if (g == 0) out[64 * x + 16 * e + r] = (u16)pk2(o, o);
      if (b == 192 && lane == 0) st4(rs, WS_EPOCH, tag);
      STAMP(10);
    }
  }
  STAMP_DUMP();
  asm volatile(".Lfb_code_end:");
}

extern "C" void kernel_launch(void* const* d_in, const int* in_sizes, int n_in, void* d_out, int out_size, void* d_ws, size_t ws_size, hipStream_t stream) {
  (void)hipFuncSetAttribute((const void*)fused_block, hipFuncAttributeMaxDynamicSharedMemorySize, L_BYTES);
  fused_block<<<256, 512, L_BYTES, stream>>>((const u16*)d_in[2], (const u16*)d_in[3], (const u16*)d_in[0], (unsigned char*)d_ws, (const u16*)d_in[1],
                                             (const u16*)d_in[4], (const u16*)d_in[5], (const u16*)d_in[7], (const u16*)d_in[8], (const u16*)d_in[9],
                                             (const u16*)d_in[10], (u16*)d_out);
}
```

```cpp
#include <hip/hip_runtime.h>
#include <hip/hip_bf16.h>

#define GAS __attribute__((address_space(1)))
#define LAS __attribute__((address_space(3)))
#define DI __device__ __forceinline__
typedef unsigned short u16;
typedef unsigned long long u64;
typedef unsigned v4u __attribute__((ext_vector_type(4)));
typedef unsigned v2u __attribute__((ext_vector_type(2)));
typedef float f32x4 __attribute__((ext_vector_type(4)));
typedef float f32x2 __attribute__((ext_vector_type(2)));
typedef short bf16x8 __attribute__((ext_vector_type(8)));
typedef short s16x4 __attribute__((ext_vector_type(4)));
typedef __bf16 bf16x2 __attribute__((ext_vector_type(2)));

constexpr int WS_EPOCH = 0;
constexpr int WS_Q1 = 4096;
constexpr int WS_Q2 = WS_Q1 + 262144;
constexpr int WS_ML = WS_Q2 + 65536;
constexpr int WS_A2 = WS_ML + 65536;
constexpr int WS_B = WS_A2 + 65536;
constexpr int WS_C = WS_B + 16384;
constexpr int WS_A = 1 << 20;
constexpr int WS_BYTES = WS_A + (4 << 20);
constexpr int CP = 1040;
constexpr int L_C = 0;
constexpr int L_Q = 128 * CP;
constexpr int L_P = L_Q + 16 * CP;
constexpr int PP = 272;
constexpr int L_MX = L_P + 16 * PP;
constexpr int L_LS = L_MX + 256;
constexpr int L_BN = L_LS + 1024;
constexpr int L_PART = L_BN + 2048;
constexpr int L_STAT = L_PART + 4096;
constexpr int L_FLAG = L_STAT + 64;
constexpr int L_BYTES = L_FLAG + 64;
static_assert(L_BYTES <= 163840, "LDS");
constexpr unsigned SENT = 0xFFFFFFFFu;
constexpr unsigned SPIN_CAP = 1u << 15;
#define INFL 6
#define QD0 6
#define QD1 10
#define PRE 2
constexpr float LOG2E = 1.4426950408889634f;

DI float bflo(unsigned u) { return __builtin_bit_cast(float, u << 16); }
DI float bfhi(unsigned u) { return __builtin_bit_cast(float, u & 0xFFFF0000u); }
DI unsigned pk2(float a, float b) { f32x2 f = {a, b}; return __builtin_bit_cast(unsigned, __builtin_convertvector(f, bf16x2)); }
DI float rbf(float a) { return bflo(pk2(a, a)); }
DI unsigned fu(float a) { return __builtin_bit_cast(unsigned, a); }
DI float uf(unsigned a) { return __builtin_bit_cast(float, a); }
DI float vmax(float a, float b) { return __builtin_elementwise_maximum(a, b); }
DI float rcp(float a) { return __builtin_amdgcn_rcpf(a); }
DI float ex2(float a) { return __builtin_amdgcn_exp2f(a); }
DI unsigned nosent(unsigned a) { return a < 0xFFFFFFFEu ? a : 0xFFFFFFFEu; }
DI f32x4 mfma(v4u a, v4u b, f32x4 c) { return __builtin_amdgcn_mfma_f32_16x16x32_bf16(__builtin_bit_cast(bf16x8, a), __builtin_bit_cast(bf16x8, b), c, 0, 0, 0); }
DI float dot2(unsigned a, unsigned b, float c) { return __builtin_amdgcn_fdot2_f32_bf16(__builtin_bit_cast(bf16x2, a), __builtin_bit_cast(bf16x2, b), c, false); }
DI float rs32(float a, float b) { auto r = __builtin_amdgcn_permlane32_swap(fu(a), fu(b), false, false); return uf(r[0]) + uf(r[1]); }
DI float rs16(float a, float b) { auto r = __builtin_amdgcn_permlane16_swap(fu(a), fu(b), false, false); return uf(r[0]) + uf(r[1]); }
DI float mx32(float a) { auto r = __builtin_amdgcn_permlane32_swap(fu(a), fu(a), false, false); return vmax(uf(r[0]), uf(r[1])); }
DI float mx16(float a) { auto r = __builtin_amdgcn_permlane16_swap(fu(a), fu(a), false, false); return vmax(uf(r[0]), uf(r[1])); }
template <int CTRL> DI float dpp(float a) { return uf((unsigned)__builtin_amdgcn_update_dpp(0, (int)fu(a), CTRL, 0xF, 0xF, true)); }
DI float row_sum(float a) { a += dpp<0x128>(a); a += dpp<0x124>(a); a += dpp<0x122>(a); a += dpp<0x121>(a); return a; }
DI v4u ldnt(const void* p) { return __builtin_nontemporal_load((const GAS v4u*)p); }
DI v4u ld16(__amdgpu_buffer_rsrc_t rs, int off) { return __builtin_amdgcn_raw_buffer_load_b128(rs, off, 0, 16); }
DI unsigned ld4(__amdgpu_buffer_rsrc_t rs, int off) { return __builtin_amdgcn_raw_buffer_load_b32(rs, off, 0, 16); }
DI void st16(__amdgpu_buffer_rsrc_t rs, int off, v4u v) { __builtin_amdgcn_raw_buffer_store_b128(v, rs, off, 0, 16); }
DI void st4(__amdgpu_buffer_rsrc_t rs, int off, unsigned v) { __builtin_amdgcn_raw_buffer_store_b32(v, rs, off, 0, 16); }
DI unsigned umax4(v4u v) { return max(max(v.x, v.y), max(v.z, v.w)); }
DI void bar() { asm volatile("s_waitcnt lgkmcnt(0)\n\ts_barrier" ::: "memory"); }
#define FENCE() asm volatile("" ::: "memory")
#define SCHED() __builtin_amdgcn_sched_barrier(0)
#define VMWAIT(N) asm volatile("s_waitcnt vmcnt(%0)" ::"i"(N) : "memory")
template <bool NT> DI void dma16(const void* base, unsigned voff, unsigned lds) {
  unsigned keep;
  if (NT) asm volatile("s_mov_b32 %0, m0\n\ts_mov_b32 m0, %3\n\ts_nop 0\n\tglobal_load_lds_dwordx4 %1, %2 nt\n\ts_mov_b32 m0, %0" : "=&s"(keep) : "v"(voff), "s"(base), "s"(lds) : "memory");
  else asm volatile("s_mov_b32 %0, m0\n\ts_mov_b32 m0, %3\n\ts_nop 0\n\tglobal_load_lds_dwordx4 %1, %2\n\ts_mov_b32 m0, %0" : "=&s"(keep) : "v"(voff), "s"(base), "s"(lds) : "memory");
}
DI v4u lds16(LAS unsigned char* L, int off) { return *(LAS v4u*)(L + off); }
DI v4u trfrag(LAS unsigned char* L, int off0, int off1) {
  s16x4 a = __builtin_amdgcn_ds_read_tr16_b64_v4i16((LAS s16x4*)(L + off0));
  s16x4 b = __builtin_amdgcn_ds_read_tr16_b64_v4i16((LAS s16x4*)(L + off1));
  v2u x = __builtin_bit_cast(v2u, a), y = __builtin_bit_cast(v2u, b);
  return v4u{x.x, x.y, y.x, y.y};
}
#define STAMP(k)
#define STAMP_DUMP()

__global__ void __launch_bounds__(512, 2)
fused_block(const u16* __restrict__ wq, const u16* __restrict__ wuk, const u16* __restrict__ xin, unsigned char* ws,
            const u16* __restrict__ ckv, const u16* __restrict__ wuv, const u16* __restrict__ wo, const u16* __restrict__ w1,
            const u16* __restrict__ b1, const u16* __restrict__ w2, const u16* __restrict__ b2, u16* __restrict__ out) {
  extern __shared__ __attribute__((aligned(16))) unsigned char smem[];
  LAS unsigned char* L = (LAS unsigned char*)smem;
  volatile LAS unsigned* FL = (volatile LAS unsigned*)(L + L_FLAG);
  const unsigned lbase = (unsigned)(size_t)L;
  const int b = blockIdx.x, lane = threadIdx.x & 63, w = __builtin_amdgcn_readfirstlane(threadIdx.x >> 6);
  const int x = b & 7, hg = (b >> 3) & 1, i = b >> 4;
  const int g = lane >> 4, r = lane & 15;
  const int h = 16 * hg + 2 * x + (i >> 3), j = i & 7;
  STAMP(0);
  asm volatile("" :: "s"(wq), "s"(wuk), "s"(xin), "s"(ws), "s"(ckv), "s"(wuv), "s"(wo), "s"(w1), "s"(b1), "s"(w2), "s"(b2), "s"(out));
  v4u wa[4], xa; v2u xr; unsigned tag;
  if (w < 4) {
    const char* base = (const char*)wq + (size_t)j * (64 * 8192) + h * 256;
    const unsigned off = (lane >> 2) * (4 * 8192) + w * 64 + (lane & 3) * 16;
    asm volatile("global_load_dwordx4 %4, %12, %14\n\tglobal_load_dwordx2 %5, %13, %14\n\t"
                 "global_load_dwordx4 %0, %7, %11 nt\n\tglobal_load_dwordx4 %1, %8, %11 nt\n\tglobal_load_dwordx4 %2, %9, %11 nt\n\tglobal_load_dwordx4 %3, %10, %11 nt\n\t"
                 "global_load_dword %6, %15, %16 sc1"
                 : "=&v"(wa[0]), "=&v"(wa[1]), "=&v"(wa[2]), "=&v"(wa[3]), "=&v"(xa), "=&v"(xr), "=&v"(tag)
                 : "v"(off), "v"(off + 8192), "v"(off + 2 * 8192), "v"(off + 3 * 8192), "s"(base),
                   "v"(lane * 16), "v"((64 * j + 4 * (lane >> 2)) * 2), "s"(xin), "v"(WS_EPOCH), "s"(ws) : "memory");
  }
  if (threadIdx.x == 256) *(volatile LAS v4u*)(L + L_FLAG) = v4u{0u, 0u, 0u, 0u};
  u64 pc0; int clen;
  asm volatile("s_getpc_b64 %0\n\t.Lpf_here%=:\n\ts_mov_b32 %1, .Lfb_code_end-.Lpf_here%=\n\ts_max_i32 %1, %1, .Lfb_code_end2-.Lpf_here%=" : "=s"(pc0), "=s"(clen) :: "scc");
  bar();
  if (w >= 4) {
    if (w < 6) {
      unsigned keep;
      const int off = max(min(((w - 4) * 64 + lane) * 128, clen - 4), 0);
      asm volatile("s_mov_b32 %0, m0\n\ts_mov_b32 m0, %3\n\ts_nop 0\n\tglobal_load_lds_dword %1, %2\n\ts_mov_b32 m0, %0" : "=&s"(keep) : "v"(off), "s"(pc0), "s"(lbase + L_PART + w * 256) : "memory");
    } else {
      const char* base = (const char*)wuk + (size_t)(64 * j + 32 * (w - 6)) * 8192 + h * 256;
      unsigned d2 = lbase + L_Q + (w - 6) * (8 * CP);
#pragma unroll 1
      for (int k = 0; k < 8; k++) { dma16<true>(base, g * 8192 + ((r ^ ((4 * k + g) & 15)) * 16), d2); base += 4 * 8192; d2 += 1024; }
    }
  }
  const __amdgpu_buffer_rsrc_t rs = __builtin_amdgcn_make_buffer_rsrc(ws, 0, WS_BYTES, 0x00020000);
  const int n = i * 8 + x;
  const bool is_head = i < 8, is_wo = (i >> 2) == 2;
  const int fp = (i - 12) * 8 + x, f = 2 * fp + hg;
  const bool is_f = i >= 12 && fp < 28;
  const int hh = 16 * hg + 2 * x + (i >> 2), q4 = i & 3;
  const int cg = ((i - 8) << 1) | hg;
  const char* wb0; const char* wb1; unsigned wo0, wo1, ws0, ws1;
  if (is_head) {
    wb0 = (const char*)wuv + (size_t)(128 * q4) * 8192 + hh * 256 + (w & 3) * 64; wb1 = wb0 + 64 * 8192;
    wo0 = wo1 = (lane >> 2) * 8192 + (lane & 3) * 16; ws0 = ws1 = 16 * 8192;
  } else if (is_wo) {
    wb0 = (const char*)wo + ((size_t)(16 * (w >> 2) + 2 * x + ((w >> 1) & 1)) * 128 + (w & 1) * 64) * 1024 + cg * 128; wb1 = wb0 + 32 * 1024;
    wo0 = wo1 = (lane >> 3) * 1024 + (lane & 7) * 16; ws0 = ws1 = 8 * 1024;
  } else {
    const int fc = is_f ? f : 0;
    wb0 = (const char*)w1 + (size_t)(64 * w) * 3584 + fc * 64; wo0 = (lane >> 2) * 3584 + (lane & 3) * 16; ws0 = 16 * 3584;
    wb1 = (const char*)w2 + (size_t)(32 * fc) * 1024 + w * 128; wo1 = (lane >> 3) * 1024 + (lane & 7) * 16; ws1 = 8 * 1024;
  }
  const int lw = L_C + w * 8192;
  int w1_ = w;
  asm volatile("" : "+s"(w1_));
  if (__builtin_expect(w1_ < 4, 1)) {
    const char* src = (const char*)ckv + (size_t)(n * 128 + w * 32) * 1024;
    unsigned dst = lbase + L_C + w * 32 * CP;
#pragma unroll
    for (int k = 0; k < PRE; k++) { dma16<true>(src, lane * 16, dst); src += 1024; dst += CP; }
    STAMP(12);
    asm volatile("s_waitcnt vmcnt(%2)" : "+v"(xa), "+v"(xr) : "i"(PRE + 5) : "memory");
    STAMP(13);
    {
      float xv[8] = {bflo(xa.x), bfhi(xa.x), bflo(xa.y), bfhi(xa.y), bflo(xa.z), bfhi(xa.z), bflo(xa.w), bfhi(xa.w)};
      const float x0 = uf(__builtin_amdgcn_readfirstlane(fu(xv[0])));
#pragma unroll
      for (int k = 0; k < 8; k++) xv[k] -= x0;
      const float s1 = ((xv[0] + xv[1]) + (xv[2] + xv[3])) + ((xv[4] + xv[5]) + (xv[6] + xv[7]));
      const float s2 = (xv[0] * xv[0] + xv[1] * xv[1] + xv[2] * xv[2] + xv[3] * xv[3]) + (xv[4] * xv[4] + xv[5] * xv[5] + xv[6] * xv[6] + xv[7] * xv[7]);
      float ss = rs32(s1, s2);
      ss = rs16(ss, ss);
      ss = row_sum(ss);
      const float md = uf(__builtin_amdgcn_readlane(fu(ss), 0)) * (1.f / 512.f);
      const float m2 = uf(__builtin_amdgcn_readlane(fu(ss), 32)) * (1.f / 512.f);
      const float mean = rbf(x0 + md);
      const float var = rbf(vmax(m2 - md * md, 0.f));
      const float rden = rcp(rbf(__builtin_amdgcn_sqrtf(rbf(var + 1e-5f))));
      const unsigned xm01 = pk2(bflo(xr.x) - mean, bfhi(xr.x) - mean), xm23 = pk2(bflo(xr.y) - mean, bfhi(xr.y) - mean);
      unsigned xn01 = pk2(bflo(xm01) * rden, bfhi(xm01) * rden);
      unsigned xn23 = pk2(bflo(xm23) * rden, bfhi(xm23) * rden);
      int qo = WS_Q1 + (h * 8 + j) * 1024 + (16 * w + 4 * (lane & 3) + g) * 16;
      asm volatile("s_waitcnt vmcnt(%8)" : "+v"(wa[0]), "+v"(wa[1]), "+v"(wa[2]), "+v"(wa[3]), "+v"(tag), "+v"(xn01), "+v"(xn23), "+v"(qo) : "i"(PRE) : "memory");
      STAMP(14);
      tag = (__builtin_amdgcn_readfirstlane(tag) + 1u) | 0x80000000u;
      float a[8];
#pragma unroll
      for (int k = 0; k < 4; k++) {
        a[2 * k] = dot2(__builtin_amdgcn_perm(wa[1][k], wa[0][k], 0x05040100), xn01, dot2(__builtin_amdgcn_perm(wa[3][k], wa[2][k], 0x05040100), xn23, 0.f));
        a[2 * k + 1] = dot2(__builtin_amdgcn_perm(wa[1][k], wa[0][k], 0x07060302), xn01, dot2(__builtin_amdgcn_perm(wa[3][k], wa[2][k], 0x07060302), xn23, 0.f));
      }
      const float c0 = rs32(a[0], a[4]), c1 = rs32(a[1], a[5]), c2 = rs32(a[2], a[6]), c3 = rs32(a[3], a[7]);
      float d0 = rs16(c0, c2), d1 = rs16(c1, c3);
      d0 += dpp<0x128>(d0); d1 += dpp<0x128>(d1);
      d0 += dpp<0x124>(d0); d1 += dpp<0x124>(d1);
      if ((lane & 12) == 0) st16(rs, qo, v4u{fu(d0), tag, fu(d1), tag});
      if (w == 3) FL[0] = tag;
    }
    STAMP(1);
    {
      int k = PRE;
#pragma unroll 1
      for (; k < INFL; k++) { dma16<true>(src, lane * 16, dst); src += 1024; dst += CP; }
#pragma unroll 1
      for (; k < 32; k++) {
        if (__builtin_amdgcn_readfirstlane(FL[2]) != 0u) break;
        VMWAIT(INFL - 1);
        dma16<true>(src, lane * 16, dst); src += 1024; dst += CP;
      }
#pragma unroll 1
      for (; k < 32; k++) { dma16<true>(src, lane * 16, dst); src += 1024; dst += CP; }
      if (w == 0) FL[3] = 1u;
    }
    STAMP(2);
    const int prow = 8 * (r >> 3) + (((r & 3) << 1) | ((r >> 2) & 1));
    const int abase = L_C + (32 * w + prow) * CP + g * 256;
    v4u af[2][16];
    VMWAIT(16);
#pragma unroll
    for (int s = 0; s < 16; s++) af[0][s] = lds16(L, abase + s * 16);
    VMWAIT(0);
    STAMP(3);
#pragma unroll
    for (int s = 0; s < 16; s++) af[1][s] = lds16(L, abase + 16 * CP + s * 16);
    bar();
    STAMP(4);
    f32x4 d0 = {0.f, 0.f, 0.f, 0.f}, d1 = {0.f, 0.f, 0.f, 0.f};
    v4u qf[16], wv[8];
#pragma unroll
    for (int s = 0; s < 16; s++) qf[s] = lds16(L, L_Q + r * CP + g * 256 + s * 16);
    SCHED();
#pragma unroll
    for (int k = 0; k < 4; k++) wv[k] = ldnt(wb0 + (wo0 + k * ws0));
#pragma unroll
    for (int k = 0; k < 4; k++) wv[4 + k] = ldnt(wb1 + (wo1 + k * ws1));
    SCHED();
#pragma unroll
    for (int s = 0; s < 16; s++) {
      d0 = mfma(af[0][s], qf[s], d0);
      d1 = mfma(af[1][s], qf[s], d1);
    }
    float sc[8];
    {
      const float c = 0.08838834764831845f;
      const unsigned r0 = pk2(d0.x, d0.y), r1 = pk2(d0.z, d0.w), r2 = pk2(d1.x, d1.y), r3 = pk2(d1.z, d1.w);
      const unsigned t0 = pk2(bflo(r0) * c, bfhi(r0) * c), t1 = pk2(bflo(r1) * c, bfhi(r1) * c), t2 = pk2(bflo(r2) * c, bfhi(r2) * c), t3 = pk2(bflo(r3) * c, bfhi(r3) * c);
      sc[0] = bflo(t0); sc[1] = bfhi(t0); sc[2] = bflo(t1); sc[3] = bfhi(t1); sc[4] = bflo(t2); sc[5] = bfhi(t2); sc[6] = bflo(t3); sc[7] = bfhi(t3);
    }
    float m = vmax(vmax(vmax(vmax(sc[0], sc[1]), sc[2]), vmax(vmax(sc[3], sc[4]), sc[5])), vmax(sc[6], sc[7]));
    m = mx32(mx16(m));
    *(LAS float*)(L + L_MX + r * 16 + w * 4) = m;
    STAMP(5);
    bar();
    const f32x4 mm = *(LAS f32x4*)(L + L_MX + r * 16);
    m = vmax(vmax(vmax(mm.x, mm.y), mm.z), mm.w);
    const float ml = m * LOG2E;
#pragma unroll
    for (int k = 0; k < 8; k++) sc[k] = ex2(sc[k] * LOG2E - ml);
    *(LAS v2u*)(L + L_P + r * PP + (32 * w + 4 * g) * 2) = v2u{pk2(sc[0], sc[1]), pk2(sc[2], sc[3])};
    *(LAS v2u*)(L + L_P + r * PP + (32 * w + 16 + 4 * g) * 2) = v2u{pk2(sc[4], sc[5]), pk2(sc[6], sc[7])};
    *(LAS float*)(L + L_LS + r * 64 + (w * 4 + g) * 4) = ((sc[0] + sc[1]) + (sc[2] + sc[3])) + ((sc[4] + sc[5]) + (sc[6] + sc[7]));
    bar();
    STAMP(6);
    if (w == 0) {
      const f32x4 l4 = *(LAS f32x4*)(L + L_LS + r * 64 + g * 16);
      float l = (l4.x + l4.y) + (l4.z + l4.w);
      l = rs16(l, l); l = rs32(l, l);
      if (g == 0) st16(rs, WS_ML + (hg * 128 + n) * 256 + r * 16, v4u{fu(ml), tag, fu(l), tag});
    }
#pragma unroll
    for (int k = 0; k < 8; k++) *(LAS v4u*)(L + lw + k * 1024 + lane * 16) = wv[k];
    STAMP(7);
  } else {
    const int v = w - 4;
    if (v < 2) {
      unsigned sp = 0;
      while ((tag = __builtin_amdgcn_readfirstlane(FL[0])) == 0u && ++sp < (SPIN_CAP << 4)) __builtin_amdgcn_s_sleep(1);
      if (v == 0) __builtin_amdgcn_s_sleep(QD0); else __builtin_amdgcn_s_sleep(QD0 + QD1);
      STAMP(1);
      for (sp = 0; __builtin_amdgcn_readfirstlane(FL[1]) == 0u; sp++) {
        FENCE();
        v4u p[8];
#pragma unroll
        for (int k = 0; k < 8; k++) p[k] = ld16(rs, WS_Q1 + (h * 8 + k) * 1024 + lane * 16);
        unsigned bad = 0;
#pragma unroll
        for (int k = 0; k < 8; k++) bad |= (p[k].y ^ tag) | (p[k].w ^ tag);
        if (__all(bad == 0) || sp > SPIN_CAP) {
          const float q0 = ((uf(p[0].x) + uf(p[1].x)) + (uf(p[2].x) + uf(p[3].x))) + ((uf(p[4].x) + uf(p[5].x)) + (uf(p[6].x) + uf(p[7].x)));
          const float q1 = ((uf(p[0].z) + uf(p[1].z)) + (uf(p[2].z) + uf(p[3].z))) + ((uf(p[4].z) + uf(p[5].z)) + (uf(p[6].z) + uf(p[7].z)));
          *(volatile LAS unsigned*)(L + L_BN + lane * 4) = pk2(q0, q1);
          FL[1] = 1u;
          break;
        }
      }
      STAMP(2);
      for (sp = 0; __builtin_amdgcn_readfirstlane(FL[2]) == 0u && sp < (SPIN_CAP << 4); sp++) __builtin_amdgcn_s_sleep(1);
      const v4u sv = {SENT, SENT, SENT, SENT};
#pragma unroll
      for (int k = 0; k < 8; k++) st16(rs, WS_A + (hg * 128 + n) * 16384 + (v * 8 + k) * 1024 + lane * 16, sv);
      if (is_head) { if (lane < 16) st16(rs, WS_A2 + (hh * 4 + q4) * 512 + v * 256 + lane * 16, sv); }
      else if (is_wo) { if (lane < 8) st16(rs, WS_B + (cg * 8 + x) * 256 + v * 128 + lane * 16, sv); }
      else if (is_f) { st16(rs, WS_C + ((v * 8 + (lane >> 3)) * 64 + f) * 128 + (lane & 7) * 16, sv); }
    } else {
      VMWAIT(0);
      v4u kf[2][4];
#pragma unroll
      for (int T = 0; T < 2; T++)
#pragma unroll
        for (int s = 0; s < 4; s++) kf[T][s] = lds16(L, L_Q + (v - 2) * (8 * CP) + (16 * T + r) * 256 + (((4 * s + g) ^ r) * 16));
      STAMP(12);
      v4u qf[4];
      for (unsigned sp = 0;; sp++) {
        const unsigned fl = FL[1];
#pragma unroll
        for (int s = 0; s < 4; s++) qf[s] = *(volatile LAS v4u*)(L + L_BN + s * 64 + g * 16);
        tag = FL[0];
        if (__builtin_amdgcn_readfirstlane(fl) != 0u || sp > (SPIN_CAP << 4)) break;
      }
      tag = __builtin_amdgcn_readfirstlane(tag);
      f32x4 o0 = {0.f, 0.f, 0.f, 0.f}, o1 = {0.f, 0.f, 0.f, 0.f};
#pragma unroll
      for (int s = 0; s < 4; s++) { o0 = mfma(kf[0][s], qf[s], o0); o1 = mfma(kf[1][s], qf[s], o1); }
      const f32x4 o = (r & 1) ? o1 : o0;
      if (r < 2) st16(rs, WS_Q2 + (h * 8 + j) * 256 + ((2 * (v - 2) + r) * 4 + g) * 16, v4u{pk2(o.x, o.y), tag, pk2(o.z, o.w), tag});
      FENCE();
      if (lane == 0) __hip_atomic_fetch_or((LAS unsigned*)(L + L_FLAG + 8), 0x80000000u >> (v - 2), __ATOMIC_RELAXED, __HIP_MEMORY_SCOPE_WORKGROUP);
      STAMP(3);
    }
    for (unsigned sp = 0; (__builtin_amdgcn_readfirstlane(FL[2]) != 0xC0000000u || __builtin_amdgcn_readfirstlane(FL[3]) == 0u) && sp < (SPIN_CAP << 4); sp++) __builtin_amdgcn_s_sleep(1);
    STAMP(4);
    for (unsigned sp = 0;; sp++) {
      FENCE();
      v4u p[8];
#pragma unroll
      for (int k = 0; k < 8; k++) p[k] = ld16(rs, WS_Q2 + hg * 32768 + (v * 8 + k) * 1024 + lane * 16);
      unsigned bad = 0;
#pragma unroll
      for (int k = 0; k < 8; k++) {
        bad |= (p[k].y ^ tag) | (p[k].w ^ tag);
        *(LAS v2u*)(L + L_Q + (4 * v + (k >> 1)) * CP + (k & 1) * 512 + lane * 8) = v2u{p[k].x, p[k].z};
      }
      if (__all(bad == 0) || sp > SPIN_CAP) break;
    }
    STAMP(5);
    bar();
    __builtin_amdgcn_s_sleep(3);
    v4u cf[4][8];
    {
      const int base = L_C + (8 * g + 2 * (r >> 2)) * CP + (128 * v + 4 * (r & 3)) * 2;
#pragma unroll
      for (int t = 0; t < 4; t++)
#pragma unroll
        for (int T = 0; T < 8; T++) cf[t][T] = trfrag(L, base + t * 32 * CP + T * 32, base + t * 32 * CP + CP + T * 32);
    }
    bar();
    bar();
    STAMP(6);
    f32x4 acc[8];
#pragma unroll
    for (int T = 0; T < 8; T++) acc[T] = f32x4{0.f, 0.f, 0.f, 0.f};
    v4u pf[4];
#pragma unroll
    for (int t = 0; t < 4; t++) pf[t] = lds16(L, L_P + r * PP + t * 64 + g * 16);
    SCHED();
#pragma unroll
    for (int t = 0; t < 4; t++)
#pragma unroll
      for (int T = 0; T < 8; T++) acc[T] = mfma(pf[t], cf[t][T], acc[T]);
#pragma unroll
    for (int k = 0; k < 4; k++) {
      const v4u o = {nosent(pk2(acc[0][k], acc[1][k])), nosent(pk2(acc[2][k], acc[3][k])), nosent(pk2(acc[4][k], acc[5][k])), nosent(pk2(acc[6][k], acc[7][k]))};
      st16(rs, WS_A + (hg * 128 + n) * 16384 + v * 4096 + k * 1024 + lane * 16, o);
    }
    STAMP(7);
    if (is_wo || is_f) {
#pragma unroll 1
      for (int k = 0; k < 4; k++) dma16<true>(wb0, wo0 + k * ws0, lbase + lw + k * 1024);
#pragma unroll 1
      for (int k = 0; k < 4; k++) dma16<true>(wb1, wo1 + k * ws1, lbase + lw + 4096 + k * 1024);
      VMWAIT(0);
    }
    asm volatile(".Lfb_code_end2:");
  }
  STAMP(8);
  const int q = r >> 2, p = r & 3;
  if (is_head) {
    v4u wf[2][4];
    if (w < 4) {
      FENCE();
#pragma unroll
      for (int T = 0; T < 2; T++)
#pragma unroll
        for (int s = 0; s < 4; s++) { const int o = lw + (16 * q + 4 * s + g) * 64 + (16 * T + 4 * p) * 2; wf[T][s] = trfrag(L, o, o + 64 * 64); }
    }
    const int hl = hh & 15;
    v4u ml;
    for (unsigned sp = 0;; sp++) {
      FENCE();
      ml = ld16(rs, WS_ML + (hg * 128 + 16 * w + 4 * p + g) * 256 + hl * 16);
      if (__all(((ml.y ^ tag) | (ml.w ^ tag)) == 0) || sp > SPIN_CAP) break;
    }
    STAMP(11);
    v4u pl[4];
    const int po = WS_A + (hg * 128 + 16 * w + g) * 16384 + q4 * 4096 + (hl & 3) * 1024 + (hl >> 2) * 256 + r * 16;
#pragma unroll
    for (int k = 0; k < 4; k++) pl[k] = ld16(rs, po + k * 65536);
    SCHED();
    float mw = uf(ml.x);
    mw = vmax(mw, dpp<0xB1>(mw)); mw = vmax(mw, dpp<0x4E>(mw));
    mw = mx32(mx16(mw));
    const float ew = ex2(uf(ml.x) - mw);
    float lsum = ew * uf(ml.z);
    lsum += dpp<0xB1>(lsum); lsum += dpp<0x4E>(lsum);
    lsum = rs16(lsum, lsum); lsum = rs32(lsum, lsum);
    if (lane == 0) *(LAS f32x2*)(L + L_STAT + w * 8) = f32x2{mw, lsum};
    const float wg0 = dpp<0x00>(ew), wg1 = dpp<0x55>(ew), wg2 = dpp<0xAA>(ew), wg3 = dpp<0xFF>(ew);
    SCHED();
    for (unsigned sp = 0; !__all(max(max(umax4(pl[0]), umax4(pl[1])), max(umax4(pl[2]), umax4(pl[3]))) != SENT) && sp < SPIN_CAP; sp++) {
      FENCE();
#pragma unroll
      for (int k = 0; k < 4; k++) pl[k] = ld16(rs, po + k * 65536);
    }
    STAMP(9);
    float a[8];
#pragma unroll
    for (int t = 0; t < 4; t++) {
      a[2 * t] = wg0 * bflo(pl[0][t]) + wg1 * bflo(pl[1][t]) + wg2 * bflo(pl[2][t]) + wg3 * bflo(pl[3][t]);
      a[2 * t + 1] = wg0 * bfhi(pl[0][t]) + wg1 * bfhi(pl[1][t]) + wg2 * bfhi(pl[2][t]) + wg3 * bfhi(pl[3][t]);
    }
    const float c0 = rs32(a[0], a[4]), c1 = rs32(a[1], a[5]), c2 = rs32(a[2], a[6]), c3 = rs32(a[3], a[7]);
    *(LAS v2u*)(L + L_Q + (2 * w + (g & 1)) * PP + (8 * r + 4 * (g >> 1)) * 2) = v2u{pk2(c0, c1), pk2(c2, c3)};
    bar();
    if (w < 4) {
      v4u of[4];
#pragma unroll
      for (int s = 0; s < 4; s++) of[s] = lds16(L, L_Q + r * PP + s * 64 + g * 16);
      const f32x4 st = *(LAS f32x4*)(L + L_STAT + g * 16);
      SCHED();
      f32x4 d0 = {0.f, 0.f, 0.f, 0.f}, d1 = {0.f, 0.f, 0.f, 0.f};
#pragma unroll
      for (int s = 0; s < 4; s++) {
        d0 = mfma(of[s], wf[0][s], d0);
        d1 = mfma(of[s], wf[1][s], d1);
      }
      const float gm = mx32(mx16(vmax(st.x, st.z)));
      const float e0 = ex2(st.x - gm), e1 = ex2(st.z - gm);
      float lt = e0 * st.y + e1 * st.w;
      lt = rs16(lt, lt); lt = rs32(lt, lt);
      float vv = rs32(e0 * (d0.x + d0.y) + e1 * (d0.z + d0.w), e0 * (d1.x + d1.y) + e1 * (d1.z + d1.w));
      vv = rs16(vv, vv) * rcp(lt);
      if ((g & 1) == 0) st4(rs, WS_A2 + (hh * 4 + q4) * 512 + (32 * w + 16 * (g >> 1) + r) * 4, nosent(fu(vv)));
      STAMP(10);
    }
  } else if (is_wo) {
    FENCE();
    v4u wf[2][4];
#pragma unroll
    for (int s = 0; s < 2; s++)
#pragma unroll
      for (int T = 0; T < 4; T++) { const int o = lw + (32 * s + 8 * g + q) * 128 + (16 * T + 4 * p) * 2; wf[s][T] = trfrag(L, o, o + 4 * 128); }
    const int col = 4 * r + 2 * (g >> 1) + (g & 1);
    const int hs = w >> 1, head = 16 * (hs >> 1) + 2 * x + (hs & 1);
    for (unsigned sp = 0;; sp++) {
      FENCE();
      const unsigned t0 = ld4(rs, WS_ML + ((hs >> 1) * 128 + (4 * (hs & 1) + (lane & 3)) * 8 + x) * 256 + 4);
      if (__all(t0 == tag) || sp > SPIN_CAP) break;
    }
    STAMP(11);
    v4u pl;
    for (unsigned sp = 0;; sp++) {
      FENCE();
      pl = ld16(rs, WS_A2 + (head * 4 + g) * 512 + (w & 1) * 256 + r * 16);
      if (__all(umax4(pl) != SENT) || sp > SPIN_CAP) break;
    }
    STAMP(9);
    const float v = rs16(rs32(uf(pl.x), uf(pl.z)), rs32(uf(pl.y), uf(pl.w)));
    *(LAS u16*)(L + L_BN + w * 256 + col * 2) = (u16)pk2(v, v);
    FENCE();
    v4u af[2];
#pragma unroll
    for (int s = 0; s < 2; s++) af[s] = lds16(L, L_BN + w * 256 + s * 64 + g * 16);
    SCHED();
    f32x4 d[4];
#pragma unroll
    for (int T = 0; T < 4; T++) d[T] = mfma(af[0], wf[0][T], f32x4{0.f, 0.f, 0.f, 0.f});
#pragma unroll
    for (int T = 0; T < 4; T++) d[T] = mfma(af[1], wf[1][T], d[T]);
    *(LAS float*)(L + L_PART + (w * 64 + lane) * 4) = g == 0 ? d[0].x : g == 1 ? d[1].x : g == 2 ? d[2].x : d[3].x;
    bar();
    if (w == 0) {
      float tp[8];
#pragma unroll
      for (int k = 0; k < 8; k++) tp[k] = *(LAS float*)(L + L_PART + (k * 64 + lane) * 4);
      st4(rs, WS_B + (cg * 8 + x) * 256 + lane * 4, nosent(fu(((tp[0] + tp[1]) + (tp[2] + tp[3])) + ((tp[4] + tp[5]) + (tp[6] + tp[7])))));
      STAMP(10);
    } else if (w == 1 && i == 8) {
      const int rho = 2 * x + hg;
      const int oc = 4 * (lane & 7) + 2 * (g >> 1) + (g & 1);
      const float xc = bflo(*(const GAS u16*)(xin + 32 * rho + oc));
      const float bias = bflo(*(const GAS u16*)(b2 + 32 * rho + oc));
      for (unsigned sp = 0;; sp++) {
        FENCE();
        const unsigned t0 = ld4(rs, WS_ML + (64 + lane) * 256 + 4), t1 = ld4(rs, WS_ML + (192 + lane) * 256 + 4);
        if (__all((t0 == tag) & (t1 == tag)) || sp > SPIN_CAP) break;
      }
      v4u hp;
      for (unsigned sp = 0;; sp++) {
        FENCE();
        hp = ld16(rs, WS_B + (x * 8 + g + 4 * ((lane >> 3) & 1)) * 256 + hg * 128 + (lane & 7) * 16);
        if (__all(umax4(hp) != SENT) || sp > SPIN_CAP) break;
      }
      float h0 = uf(hp.x), h1 = uf(hp.y), h2 = uf(hp.z), h3 = uf(hp.w);
      h0 += dpp<0x128>(h0); h1 += dpp<0x128>(h1); h2 += dpp<0x128>(h2); h3 += dpp<0x128>(h3);
      const float hres = rbf(xc + rbf(rs16(rs32(h0, h2), rs32(h1, h3))));
      v4u y[7];
      for (unsigned sp = 0;; sp++) {
        FENCE();
#pragma unroll
        for (int k = 0; k < 7; k++) y[k] = ld16(rs, WS_C + rho * 8192 + k * 1024 + lane * 16);
        unsigned mxs = 0;
#pragma unroll
        for (int k = 0; k < 7; k++) mxs = max(mxs, umax4(y[k]));
        if (__all(mxs != SENT) || sp > SPIN_CAP) break;
      }
      STAMP(15);
      f32x4 ys = ((__builtin_bit_cast(f32x4, y[0]) + __builtin_bit_cast(f32x4, y[1])) + (__builtin_bit_cast(f32x4, y[2]) + __builtin_bit_cast(f32x4, y[3]))) +
                 ((__builtin_bit_cast(f32x4, y[4]) + __builtin_bit_cast(f32x4, y[5])) + __builtin_bit_cast(f32x4, y[6]));
      float yv = rs16(rs32(ys.x, ys.z), rs32(ys.y, ys.w));
      yv += dpp<0x128>(yv);
      const float o = hres + rbf(rbf(yv) + bias);
      if ((lane & 8) == 0) out[32 * rho + oc] = (u16)pk2(o, o);
      if (b == 128 && lane == 0) st4(rs, WS_EPOCH, tag);
      STAMP(10);
    }
  } else if (is_f) {
    const int col = 4 * r + 2 * (g >> 1) + (g & 1);
    const int hu = 8 * g + (r & 7);
    const float xc = bflo(*(const GAS u16*)(xin + 64 * w + col));
    const float bias = bflo(*(const GAS u16*)(b1 + 32 * f + hu));
    FENCE();
    v4u wf[2][2], w2f[4];
#pragma unroll
    for (int s = 0; s < 2; s++)
#pragma unroll
      for (int T = 0; T < 2; T++) { const int o = lw + (32 * s + 8 * g + q) * 64 + (16 * T + 4 * p) * 2; wf[s][T] = trfrag(L, o, o + 4 * 64); }
#pragma unroll
    for (int T = 0; T < 4; T++) { const int o = lw + 4096 + (8 * g + q) * 128 + (16 * T + 4 * p) * 2; w2f[T] = trfrag(L, o, o + 4 * 128); }
    for (unsigned sp = 0;; sp++) {
      FENCE();
      const unsigned t0 = ld4(rs, WS_ML + ((w & 1) * 128 + (8 + (w >> 1)) * 8 + (lane & 7)) * 256 + 4);
      if (__all(t0 == tag) || sp > SPIN_CAP) break;
    }
    STAMP(11);
    v4u p0, p1;
    for (unsigned sp = 0;; sp++) {
      FENCE();
      p0 = ld16(rs, WS_B + (w * 8 + g) * 256 + r * 16);
      p1 = ld16(rs, WS_B + (w * 8 + 4 + g) * 256 + r * 16);
      if (__all(max(umax4(p0), umax4(p1)) != SENT) || sp > SPIN_CAP) break;
    }
    STAMP(9);
    float v = rs16(rs32(uf(p0.x) + uf(p1.x), uf(p0.z) + uf(p1.z)), rs32(uf(p0.y) + uf(p1.y), uf(p0.w) + uf(p1.w)));
    v = xc + rbf(v);
    *(LAS u16*)(L + L_BN + w * 256 + col * 2) = (u16)pk2(v, v);
    FENCE();
    v4u af[2];
#pragma unroll
    for (int s = 0; s < 2; s++) af[s] = lds16(L, L_BN + w * 256 + s * 64 + g * 16);
    SCHED();
    f32x4 d[2] = {{0.f, 0.f, 0.f, 0.f}, {0.f, 0.f, 0.f, 0.f}}, ds = {0.f, 0.f, 0.f, 0.f};
#pragma unroll
    for (int s = 0; s < 2; s++) {
      if (p == 1) af[s] = v4u{0x3F803F80u, 0x3F803F80u, 0x3F803F80u, 0x3F803F80u};
      d[0] = mfma(af[s], wf[s][0], d[0]);
      d[1] = mfma(af[s], wf[s][1], d[1]);
      ds = mfma(af[s], af[s], ds);
    }
    const f32x4 dz = g == 0 ? d[0] : g == 1 ? d[1] : ds;
    if (lane <= 32) *(LAS f32x2*)(L + L_PART + lane * 80 + w * 8) = f32x2{dz.x, dz.y};
    bar();
    f32x4 tp[4], sq[4];
#pragma unroll
    for (int k = 0; k < 4; k++) { tp[k] = *(LAS f32x4*)(L + L_PART + hu * 80 + k * 16); sq[k] = *(LAS f32x4*)(L + L_PART + 32 * 80 + k * 16); }
    SCHED();
    const f32x4 t4 = (tp[0] + tp[1]) + (tp[2] + tp[3]), s4 = (sq[0] + sq[1]) + (sq[2] + sq[3]);
    const float mu = (s4.y + s4.w) * (1.f / 512.f);
    const float var = rbf(vmax((s4.x + s4.z) * (1.f / 512.f) - mu * mu, 0.f));
    const float rden = rcp(rbf(__builtin_amdgcn_sqrtf(rbf(var + 1e-5f))));
    const float uu = rbf(rbf(((t4.x + t4.z) - rbf(mu) * (t4.y + t4.w)) * rden) + bias);
    const float z = uu * (0.044715f * uu * uu + 1.f);
    const float ge = uu * rcp(1.f + ex2(-2.f * 0.7978845608028654f * LOG2E * z));
    const unsigned gp = pk2(ge, ge) & ((r & 1) ? 0xFFFF0000u : 0x0000FFFFu);
    const int dw = (r >> 1) & 3;
    const v4u ga = {dw == 0 ? gp : 0u, dw == 1 ? gp : 0u, dw == 2 ? gp : 0u, dw == 3 ? gp : 0u};
    f32x4 y[4];
#pragma unroll
    for (int T = 0; T < 4; T++) y[T] = mfma(ga, w2f[T], f32x4{0.f, 0.f, 0.f, 0.f});
    const f32x4 ya = lane < 32 ? y[0] : y[2], yb = lane < 32 ? y[1] : y[3];
    const float yo = rs16((ya.x + ya.y) + (ya.z + ya.w), (yb.x + yb.y) + (yb.z + yb.w));
    st4(rs, WS_C + ((2 * w + (lane >> 5)) * 64 + f) * 128 + (lane & 31) * 4, nosent(fu(yo)));
    STAMP(10);
  }
  STAMP_DUMP();
  asm volatile(".Lfb_code_end:");
}

#define WSEND 0
static unsigned char* ws_base(void* d_ws, size_t ws_size) { return (unsigned char*)d_ws + (WSEND ? ((ws_size - (size_t)(16 << 20)) & ~(size_t)0xFFFFF) : 0); }
extern "C" void kernel_launch(void* const* d_in, const int* in_sizes, int n_in, void* d_out, int out_size, void* d_ws, size_t ws_size, hipStream_t stream) {
  (void)hipFuncSetAttribute((const void*)fused_block, hipFuncAttributeMaxDynamicSharedMemorySize, L_BYTES);
  fused_block<<<256, 512, L_BYTES, stream>>>((const u16*)d_in[2], (const u16*)d_in[3], (const u16*)d_in[0], ws_base(d_ws, ws_size), (const u16*)d_in[1],
                                             (const u16*)d_in[4], (const u16*)d_in[5], (const u16*)d_in[7], (const u16*)d_in[8], (const u16*)d_in[9],
                                             (const u16*)d_in[10], (u16*)d_out);
}
```
